```python
import torch

def _softmax(x, axis=-1):
    return torch.softmax(x, dim=axis)
def _gelu(x):
    return torch.nn.functional.gelu(x, approximate="tanh")
def _silu(x):
    return torch.nn.functional.silu(x)
def _sigmoid(x):
    return torch.sigmoid(x)
def _layernorm(x, axis=-1, eps=1e-5):
    m = x.mean(dim=axis, keepdim=True)
    v = x.var(dim=axis, keepdim=True, unbiased=False)
    return (x - m) / torch.sqrt(v + eps)
def _scatter_add(tgt, idx, src, axis=0):
    return tgt.clone().index_add_(axis, idx, src)
def _segment_sum(src, seg, n):
    out = torch.zeros((n,) + src.shape[1:], dtype=src.dtype)
    return out.index_add_(0, seg, src)
def _pad(x, pw):
    flat = tuple(v for pair in reversed(pw) for v in pair)
    return torch.nn.functional.pad(x, flat)

kernel_name = 'concat_heads__softmax_full_mha__linear_kernel_hedgehog__prompt'

def setup_inputs():

    _w = (lambda a: (lambda t: t.to(torch.bfloat16) if t.is_floating_point() else t)(torch.as_tensor(a)))
    def _A_setup():
        B, S, D = 1, 2048, 1024
        return {
            "x": _w((lambda *s: torch.randn(*s, dtype=torch.bfloat16) * 0.2)(B, S, D)),
            "wqkv": _w((lambda *s: torch.randn(*s, dtype=torch.bfloat16) * 0.2)(D, 3 * D)),
            "wo": _w((lambda *s: torch.randn(*s, dtype=torch.bfloat16) * 0.05)(D, D)),
        }
    _A_in = _A_setup()

    def _B_setup():
        B, S, D, H = 1, 2048, 1024, 32
        Dh = D // H
        return {
            "x": _w((lambda *s: torch.randn(*s, dtype=torch.bfloat16) * 0.4)(B, S, D)),
            "wqkv": _w((lambda *s: torch.randn(*s, dtype=torch.bfloat16) * 0.4)(D, 3 * D)),
            "w_phi": _w((lambda *s: torch.randn(*s, dtype=torch.bfloat16) * 0.4)(H, Dh, Dh) * (Dh ** -0.5)),
            "wo": _w((lambda *s: torch.randn(*s, dtype=torch.bfloat16) * 0.05)(D, D)),
        }
    _B_in = _B_setup()

    return {
        "A_x": _A_in["x"],
        "A_wqkv": _A_in["wqkv"],
        "A_wo": _A_in["wo"],
        "B_x": _B_in["x"],
        "B_wqkv": _B_in["wqkv"],
        "B_w_phi": _B_in["w_phi"],
        "B_wo": _B_in["wo"],
    }

def reference(A_x, A_wqkv, A_wo, B_x, B_wqkv, B_w_phi, B_wo):
    def _A_ref(x, wqkv, wo):
        B, S, D = x.shape
        H = 32; Dh = D // H
        qkv = (lambda x, *d: x.permute(*d))((x @ wqkv).reshape(B, S, 3, H, Dh), 2, 0, 3, 1, 4)
        q, k, v = qkv[0], qkv[1], qkv[2]
        scale = Dh ** -0.5
        scores = torch.einsum("bhqd,bhkd->bhqk", q, k) * scale
        p = _softmax(scores, axis=-1)
        o = torch.einsum("bhqk,bhkd->bhqd", p, v)
        return (lambda x, *d: x.permute(*d))(o, 0, 2, 1, 3).reshape(B, S, D) @ wo

    def _B_ref(x, wqkv, w_phi, wo):
        B, S, D = x.shape
        H = 32; Dh = D // H
        qkv = (lambda x, *d: x.permute(*d))((x @ wqkv).reshape(B, S, 3, H, Dh), 2, 0, 3, 1, 4)
        q, k, v = qkv[0], qkv[1], qkv[2]
        pq = torch.einsum("bhsd,hdf->bhsf", q, w_phi)
        pk = torch.einsum("bhsd,hdf->bhsf", k, w_phi)
        phi_q = torch.concatenate([_softmax(pq, axis=-1), _softmax(-pq, axis=-1)], axis=-1)
        phi_k = torch.concatenate([_softmax(pk, axis=-1), _softmax(-pk, axis=-1)], axis=-1)
        kv = torch.einsum("bhsf,bhsd->bhfd", phi_k, v)
        z  = torch.einsum("bhsf->bhf", phi_k)
        num = torch.einsum("bhqf,bhfd->bhqd", phi_q, kv)
        den = torch.einsum("bhqf,bhf->bhq", phi_q, z)[..., None] + 1e-6
        o = num / den
        return (lambda x, *d: x.permute(*d))(o, 0, 2, 1, 3).reshape(B, S, D) @ wo

    a = _A_ref(A_x, A_wqkv, A_wo)
    b = _B_ref(B_x, B_wqkv, B_w_phi, B_wo)
    return torch.concatenate([a, b], axis=-1)
```

```cpp
#include <hip/hip_runtime.h>
#include <hip/hip_bf16.h>
#include <stdint.h>

#define DI __device__ __forceinline__
#define LDSP __attribute__((address_space(3)))
typedef __bf16 bf2 __attribute__((ext_vector_type(2)));
typedef __bf16 bf8 __attribute__((ext_vector_type(8)));
typedef __bf16 bf16v __attribute__((ext_vector_type(16)));
typedef float f2 __attribute__((ext_vector_type(2)));
typedef float f4 __attribute__((ext_vector_type(4)));
typedef float f8 __attribute__((ext_vector_type(8)));
typedef float f16v __attribute__((ext_vector_type(16)));
typedef unsigned u2 __attribute__((ext_vector_type(2)));
typedef unsigned u4 __attribute__((ext_vector_type(4)));
typedef short s4 __attribute__((ext_vector_type(4)));
typedef short s8 __attribute__((ext_vector_type(8)));
typedef LDSP char* lptr;

#define SB() __builtin_amdgcn_sched_barrier(0)
#define PIN(x) asm volatile("" : "+v"(x))
#define MFMA(a, b, c) __builtin_amdgcn_mfma_f32_32x32x16_bf16((a), (b), (c), 0, 0, 0)
#define WAIT_BAR(N) asm volatile("s_waitcnt vmcnt(" #N ") lgkmcnt(0)\n\ts_barrier" ::: "memory")

constexpr float LOG2E = 1.4426950408889634f;
constexpr float QSCALE = 0.17677669529663687f * 1.4426950408889634f;

constexpr size_t WS_QA = 0;
constexpr size_t WS_KVA = WS_QA + (4u << 20);
constexpr size_t WS_QB = WS_KVA + (8u << 20);
constexpr size_t WS_KVP = WS_QB + (4u << 20);
constexpr size_t WS_O = WS_KVP + (3u << 20);

DI unsigned pk2(float a, float b) { bf2 r = __builtin_convertvector((f2){a, b}, bf2); return __builtin_bit_cast(unsigned, r); }
DI u4 pack8(const f16v& x, int s) { return (u4){pk2(x[8 * s], x[8 * s + 1]), pk2(x[8 * s + 2], x[8 * s + 3]), pk2(x[8 * s + 4], x[8 * s + 5]), pk2(x[8 * s + 6], x[8 * s + 7])}; }
DI bf8 asbf(u4 v) { return __builtin_bit_cast(bf8, v); }
DI bf8 ld128(lptr p) { return *(const LDSP bf8*)p; }
DI s4 ldtr(lptr p) { return __builtin_amdgcn_ds_read_tr16_b64_v4i16((LDSP s4*)p); }
DI bf8 cat(s4 lo, s4 hi) { return __builtin_bit_cast(bf8, __builtin_shufflevector(lo, hi, 0, 1, 2, 3, 4, 5, 6, 7)); }
DI void glds16(const void* sbase, unsigned voff, unsigned lds) {
  asm volatile("s_mov_b32 m0, %2\n\ts_nop 0\n\tglobal_load_lds_dwordx4 %0, %1" ::"v"(voff), "s"(sbase), "s"(lds) : "memory");
}
DI float xhalf_max(float m) { auto rr = __builtin_amdgcn_permlane32_swap(__float_as_uint(m), __float_as_uint(m), false, false); return __builtin_fmaxf(__uint_as_float(rr[0]), __uint_as_float(rr[1])); }
DI float xhalf_min(float m) { auto rr = __builtin_amdgcn_permlane32_swap(__float_as_uint(m), __float_as_uint(m), false, false); return __builtin_fminf(__uint_as_float(rr[0]), __uint_as_float(rr[1])); }
DI float xhalf_sum(float m) { auto rr = __builtin_amdgcn_permlane32_swap(__float_as_uint(m), __float_as_uint(m), false, false); return __uint_as_float(rr[0]) + __uint_as_float(rr[1]); }
DI float max16(const f16v& p) {
  float a = __builtin_fmaxf(__builtin_fmaxf(p[0], p[1]), p[2]), b = __builtin_fmaxf(__builtin_fmaxf(p[3], p[4]), p[5]);
  a = __builtin_fmaxf(__builtin_fmaxf(a, p[6]), p[7]); b = __builtin_fmaxf(__builtin_fmaxf(b, p[8]), p[9]);
  a = __builtin_fmaxf(__builtin_fmaxf(a, p[10]), p[11]); b = __builtin_fmaxf(__builtin_fmaxf(b, p[12]), p[13]);
  a = __builtin_fmaxf(__builtin_fmaxf(a, p[14]), p[15]); return __builtin_fmaxf(a, b);
}
DI float min16(const f16v& p) {
  float a = __builtin_fminf(__builtin_fminf(p[0], p[1]), p[2]), b = __builtin_fminf(__builtin_fminf(p[3], p[4]), p[5]);
  a = __builtin_fminf(__builtin_fminf(a, p[6]), p[7]); b = __builtin_fminf(__builtin_fminf(b, p[8]), p[9]);
  a = __builtin_fminf(__builtin_fminf(a, p[10]), p[11]); b = __builtin_fminf(__builtin_fminf(b, p[12]), p[13]);
  a = __builtin_fminf(__builtin_fminf(a, p[14]), p[15]); return __builtin_fminf(a, b);
}
DI float sum16(const f16v& p) {
  float a = (p[0] + p[1]) + (p[2] + p[3]), b = (p[4] + p[5]) + (p[6] + p[7]), c = (p[8] + p[9]) + (p[10] + p[11]), d = (p[12] + p[13]) + (p[14] + p[15]);
  return (a + b) + (c + d);
}
DI void dual_exp(const f16v& p, f16v& ep, f16v& em, float& sp, float& sm) {
  const float mx = xhalf_max(max16(p)) * LOG2E, mn = xhalf_min(min16(p)) * LOG2E;
#pragma unroll
  for (int r = 0; r < 16; ++r) { ep[r] = __builtin_amdgcn_exp2f(__builtin_fmaf(p[r], LOG2E, -mx)); em[r] = __builtin_amdgcn_exp2f(__builtin_fmaf(p[r], -LOG2E, mn)); }
  sp = xhalf_sum(sum16(ep)); sm = xhalf_sum(sum16(em));
}

constexpr int K1_W = 65536, K1_PHI = 114688, K1_LDS = 118784;
struct Frag1 { bf8 x0, x1; s4 w[3][2]; };

__global__ __launch_bounds__(512, 2) void k1_qkv(const char* __restrict__ xa, const char* __restrict__ wa, const char* __restrict__ xb, const char* __restrict__ wb,
                                                 const char* __restrict__ wphi, char* __restrict__ ws) {
  __shared__ __attribute__((aligned(1024))) char smem[K1_LDS];
  const lptr L = (lptr)smem;
  const unsigned L0 = (unsigned)(uintptr_t)L;
  const int tid = threadIdx.x, lane = tid & 63, w = __builtin_amdgcn_readfirstlane(tid >> 6);
  const int bid = blockIdx.x, xcd = bid & 7, br = xcd >> 2, hp = (xcd & 3) * 4 + ((bid >> 3) & 3), rb = bid >> 5;
  const char* X = (br ? xb : xa) + (size_t)rb * 256 * 2048;
  const char* W = (br ? wb : wa) + hp * 128;
  unsigned xoff[4], woff[3];
#pragma unroll
  for (int i = 0; i < 4; ++i) { const int row = 32 * w + 8 * i + (lane >> 3); xoff[i] = row * 2048 + (((lane & 7) ^ ((row >> 1) & 7)) << 4); }
#pragma unroll
  for (int i = 0; i < 3; ++i) { const int k = 8 * w + (lane >> 3); woff[i] = k * 6144 + i * 2048 + (((lane & 7) ^ (((k >> 1) & 1) << 2)) << 4); }
#define DMAX(t, slot, i) glds16(X + (t) * 128, xoff[i], L0 + (slot) * 32768 + (4 * w + (i)) * 1024)
#define DMAW(t, slot, i) glds16(W + (size_t)(t) * (64 * 6144), woff[i], L0 + K1_W + (slot) * 24576 + (i) * 8192 + w * 1024)
#define DMA_ALL(t, slot) do { DMAX(t, slot, 0); DMAW(t, slot, 0); DMAX(t, slot, 1); DMAW(t, slot, 1); DMAX(t, slot, 2); DMAW(t, slot, 2); DMAX(t, slot, 3); } while (0)
  DMA_ALL(0, 0);
  if (br && w < 4) glds16(wphi + hp * 4096 + w * 1024, lane * 16, L0 + K1_PHI + w * 1024);
  DMA_ALL(1, 1);

  const int wm = w & 3, hh = w >> 2, r = lane & 31, h = lane >> 5, g1 = (lane >> 4) & 1, q = (lane >> 2) & 3, p = lane & 3;
  lptr xa_[4];
#pragma unroll
  for (int ks = 0; ks < 4; ++ks) xa_[ks] = L + (wm * 64 + r) * 128 + (((h ^ ((r >> 1) & 7)) ^ (2 * ks)) << 4);
  const lptr wa_ = L + K1_W + (8 * h + q) * 128 + (((hh * 4 + 2 * g1 + (p >> 1)) ^ (((q >> 1) & 1) << 2)) << 4) + 8 * (p & 1);

  f16v acc[2][3];
#pragma unroll
  for (int i = 0; i < 2; ++i)
#pragma unroll
    for (int j = 0; j < 3; ++j) acc[i][j] = f16v{};
  Frag1 FA, FB;
#define RX(F, m, slot, ks) F.x##m = ld128(xa_[ks] + (slot) * 32768 + (m) * 4096)
#define RW(F, sg, u, slot, ks) F.w[sg][u] = ldtr(wa_ + (slot) * 24576 + (sg) * 8192 + (ks) * 2048 + (u) * 512)
#define WF(F, sg) cat(F.w[sg][0], F.w[sg][1])
#define KSTEP(C, N, RD, slot, ks, D0, D1, D2, D3, D4, D5, D6) do { \
    if (RD) { RW(N, 0, 0, slot, ks); RW(N, 0, 1, slot, ks); } D0; SB(); \
    acc[0][0] = MFMA(WF(C, 0), C.x0, acc[0][0]); SB(); if (RD) { RX(N, 0, slot, ks); } D1; SB(); \
    acc[1][0] = MFMA(WF(C, 0), C.x1, acc[1][0]); SB(); if (RD) { RX(N, 1, slot, ks); } D2; SB(); \
    acc[0][1] = MFMA(WF(C, 1), C.x0, acc[0][1]); SB(); if (RD) { RW(N, 1, 0, slot, ks); RW(N, 1, 1, slot, ks); } D3; SB(); \
    acc[1][1] = MFMA(WF(C, 1), C.x1, acc[1][1]); SB(); if (RD) { RW(N, 2, 0, slot, ks); RW(N, 2, 1, slot, ks); } D4; SB(); \
    acc[0][2] = MFMA(C.x0, WF(C, 2), acc[0][2]); SB(); D5; SB(); \
    acc[1][2] = MFMA(C.x1, WF(C, 2), acc[1][2]); SB(); D6; SB(); } while (0)
#define NOP_ (void)0
#define STAGE(t, slot, HASNEXT, HASDMA) do { \
    KSTEP(FA, FB, true, slot, 1, NOP_, NOP_, NOP_, NOP_, NOP_, NOP_, NOP_); \
    KSTEP(FB, FA, true, slot, 2, NOP_, NOP_, NOP_, NOP_, NOP_, NOP_, NOP_); \
    KSTEP(FA, FB, true, slot, 3, NOP_, NOP_, NOP_, NOP_, NOP_, NOP_, NOP_); \
    if (HASNEXT) WAIT_BAR(0); \
    if (HASDMA) KSTEP(FB, FA, true, (slot) ^ 1, 0, DMAX((t) + 2, slot, 0), DMAW((t) + 2, slot, 0), DMAX((t) + 2, slot, 1), DMAW((t) + 2, slot, 1), DMAX((t) + 2, slot, 2), DMAW((t) + 2, slot, 2), DMAX((t) + 2, slot, 3)); \
    else KSTEP(FB, FA, HASNEXT, (slot) ^ 1, 0, NOP_, NOP_, NOP_, NOP_, NOP_, NOP_, NOP_); } while (0)
  WAIT_BAR(7);
  RW(FA, 0, 0, 0, 0); RW(FA, 0, 1, 0, 0); RX(FA, 0, 0, 0); RX(FA, 1, 0, 0); RW(FA, 1, 0, 0, 0); RW(FA, 1, 1, 0, 0); RW(FA, 2, 0, 0, 0); RW(FA, 2, 1, 0, 0);
  for (int t = 0; t < 14; t += 2) { STAGE(t, 0, true, true); STAGE(t + 1, 1, true, true); }
  STAGE(14, 0, true, false);
  STAGE(15, 1, false, false);

  const int head = hp * 2 + hh, tb0 = rb * 8 + wm * 2;
  if (br == 0) {
    const int kl = (lane & 0x33) | ((lane & 4) << 1) | ((lane & 8) >> 1);
#pragma unroll
    for (int mt = 0; mt < 2; ++mt) {
      char* qd = ws + WS_QA + (size_t)(head * 64 + tb0 + mt) * 2048;
      char* kd = ws + WS_KVA + (size_t)(head * 64 + tb0 + mt) * 4096;
      const f16v qs = acc[mt][0] * QSCALE;
#pragma unroll
      for (int s = 0; s < 2; ++s) {
        *(u4*)(qd + s * 1024 + lane * 16) = pack8(qs, s);
        *(u4*)(kd + s * 1024 + kl * 16) = pack8(acc[mt][1], s);
        *(u4*)(kd + 2048 + s * 1024 + lane * 16) = pack8(acc[mt][2], s);
      }
    }
  } else {
#pragma unroll
    for (int mt = 0; mt < 2; ++mt) {
      char* qd = ws + WS_QB + (size_t)(head * 64 + tb0 + mt) * 2048;
#pragma unroll
      for (int s = 0; s < 2; ++s) *(u4*)(qd + s * 1024 + lane * 16) = pack8(acc[mt][0], s);
    }
    bf8 wf[2];
#pragma unroll
    for (int s = 0; s < 2; ++s) { const lptr a = L + K1_PHI + hh * 2048 + (16 * s + 4 * h + q) * 64 + g1 * 32 + p * 8; wf[s] = cat(ldtr(a), ldtr(a + 512)); }
    const unsigned one2 = 0x3f803f80u;
    const unsigned op = r < 16 ? one2 : 0u, om = r < 16 ? 0u : one2;
    const bf8 onesP = asbf((u4){op, op, op, op}), onesM = asbf((u4){om, om, om, om});
    f16v kvp = f16v{}, kvm = f16v{}, zz = f16v{};
    const lptr scr = L + w * 4096;
    const lptr wr = scr + r * 64;
    const int fsw = (r >> 1) & 7;
    const lptr rd0 = scr + (4 * h + q) * 64 + (((4 * g1 + p) ^ ((2 * h + (q >> 1)) & 7)) << 3);
    const lptr rd1 = scr + (8 + 4 * h + q) * 64 + (((4 * g1 + p) ^ ((4 + 2 * h + (q >> 1)) & 7)) << 3);
#pragma unroll
    for (int mt = 0; mt < 2; ++mt) {
      f16v pk = MFMA(wf[0], asbf(pack8(acc[mt][1], 0)), f16v{});
      pk = MFMA(wf[1], asbf(pack8(acc[mt][1], 1)), pk);
      f16v ep, em; float sp, sm;
      dual_exp(pk, ep, em, sp, sm);
      ep *= __builtin_amdgcn_rcpf(sp); em *= __builtin_amdgcn_rcpf(sm);
#pragma unroll
      for (int g = 0; g < 4; ++g) {
        *(LDSP u2*)(wr + (((2 * g + h) ^ fsw) << 3)) = (u2){pk2(ep[4 * g], ep[4 * g + 1]), pk2(ep[4 * g + 2], ep[4 * g + 3])};
        *(LDSP u2*)(wr + 2048 + (((2 * g + h) ^ fsw) << 3)) = (u2){pk2(em[4 * g], em[4 * g + 1]), pk2(em[4 * g + 2], em[4 * g + 3])};
      }
      asm volatile("s_waitcnt lgkmcnt(0)" ::: "memory");
#pragma unroll
      for (int s = 0; s < 2; ++s) {
        const bf8 vf = asbf(pack8(acc[mt][2], s));
        const bf8 fp = cat(ldtr(rd0 + s * 1024), ldtr(rd1 + s * 1024)), fm = cat(ldtr(rd0 + 2048 + s * 1024), ldtr(rd1 + 2048 + s * 1024));
        kvp = MFMA(fp, vf, kvp); kvm = MFMA(fm, vf, kvm);
        zz = MFMA(fp, onesP, zz); zz = MFMA(fm, onesM, zz);
      }
      asm volatile("s_waitcnt lgkmcnt(0)" ::: "memory");
    }
    __syncthreads();
    const lptr red = L + w * 12288 + lane * 16;
#pragma unroll
    for (int g = 0; g < 4; ++g) {
      *(LDSP f4*)(red + g * 1024) = (f4){kvp[4 * g], kvp[4 * g + 1], kvp[4 * g + 2], kvp[4 * g + 3]};
      *(LDSP f4*)(red + 4096 + g * 1024) = (f4){kvm[4 * g], kvm[4 * g + 1], kvm[4 * g + 2], kvm[4 * g + 3]};
      *(LDSP f4*)(red + 8192 + g * 1024) = (f4){zz[4 * g], zz[4 * g + 1], zz[4 * g + 2], zz[4 * g + 3]};
    }
    __syncthreads();
    char* pd = ws + WS_KVP + (size_t)(head * 8 + rb) * 12288 + lane * 16;
#pragma unroll
    for (int i = 0; i < 3; ++i) {
      const int piece = wm * 3 + i;
      const lptr a = L + hh * 49152 + piece * 1024 + lane * 16;
      const f4 v = (*(LDSP f4*)a + *(LDSP f4*)(a + 12288)) + (*(LDSP f4*)(a + 24576) + *(LDSP f4*)(a + 36864));
      *(f4*)(pd + piece * 1024) = v;
    }
  }
#undef DMAX
#undef DMAW
#undef DMA_ALL
#undef RX
#undef RW
#undef WF
#undef KSTEP
#undef STAGE
}

constexpr int K2_PHI = 65536, K2_KV = K2_PHI + 2048, K2_LDS = K2_KV + 6144;
#define SMF32(a, b, c) __builtin_amdgcn_smfmac_f32_32x32x32_bf16((a), (b), (c), sidx, 0, 0)
#define SMF16(a, b, c) __builtin_amdgcn_smfmac_f32_16x16x64_bf16((a), (b), (c), sidx, 0, 0)
DI bf16v lo8(const f16v& x) { return __builtin_bit_cast(bf16v, __builtin_shufflevector(x, x, 0, 1, 2, 3, 4, 5, 6, 7)); }
DI bf16v hi8(const f16v& x) { return __builtin_bit_cast(bf16v, __builtin_shufflevector(x, x, 8, 9, 10, 11, 12, 13, 14, 15)); }
DI f16v splat16(float v) { return (f16v){v, v, v, v, v, v, v, v, v, v, v, v, v, v, v, v}; }

__global__ __launch_bounds__(512, 2) void k2_attn(const char* __restrict__ wphi, char* __restrict__ ws) {
  __shared__ __attribute__((aligned(1024))) char smem[K2_LDS];
  const lptr L = (lptr)smem;
  const unsigned L0 = (unsigned)(uintptr_t)L;
  const int tid = threadIdx.x, lane = tid & 63, w = __builtin_amdgcn_readfirstlane(tid >> 6);
  const int bid = blockIdx.x, head = (bid & 7) * 4 + ((bid >> 3) & 3), rb = bid >> 5, tb = rb * 8 + w;
  const int r = lane & 31, h = lane >> 5, g1 = (lane >> 4) & 1, q = (lane >> 2) & 3, p = lane & 3;
  const char* KV = ws + WS_KVA + (size_t)head * 64 * 4096;
  const unsigned l16 = lane * 16;
#define DMAT(T) do { glds16(KV + (T) * 16384 + w * 2048, l16, L0 + ((T) & 3) * 16384 + w * 2048); glds16(KV + (T) * 16384 + w * 2048 + 1024, l16, L0 + ((T) & 3) * 16384 + w * 2048 + 1024); } while (0)
  if (w < 2) glds16(wphi + head * 2048 + w * 1024, l16, L0 + K2_PHI + w * 1024);
  DMAT(0); DMAT(1); DMAT(2);
  bf8 qa[2], qb[2];
#pragma unroll
  for (int s = 0; s < 2; ++s) {
    qa[s] = *(const bf8*)(ws + WS_QA + (size_t)(head * 64 + tb) * 2048 + s * 1024 + l16);
    qb[s] = *(const bf8*)(ws + WS_QB + (size_t)(head * 64 + tb) * 2048 + s * 1024 + l16);
  }
  if (w < 6) {
    const char* src = ws + WS_KVP + (size_t)head * 8 * 12288 + (w >> 1) * 4096 + (w & 1) * 2048 + l16;
    f4 a = *(const f4*)src, b = *(const f4*)(src + 1024);
#pragma unroll
    for (int i = 1; i < 8; ++i) { a += *(const f4*)(src + i * 12288); b += *(const f4*)(src + i * 12288 + 1024); }
    *(LDSP u4*)(L + K2_KV + w * 1024 + l16) = (u4){pk2(a[0], a[1]), pk2(a[2], a[3]), pk2(b[0], b[1]), pk2(b[2], b[3])};
  }
  WAIT_BAR(0);
  {
    bf8 wf[2];
#pragma unroll
    for (int s = 0; s < 2; ++s) { const lptr a = L + K2_PHI + (16 * s + 4 * h + q) * 64 + g1 * 32 + p * 8; wf[s] = cat(ldtr(a), ldtr(a + 512)); }
    f16v pq = MFMA(wf[0], qb[0], f16v{});
    pq = MFMA(wf[1], qb[1], pq);
    f16v ep, em; float sp, sm;
    dual_exp(pq, ep, em, sp, sm);
    em *= sp * __builtin_amdgcn_rcpf(sm);
    f16v num = f16v{}, den = f16v{};
#pragma unroll
    for (int s = 0; s < 2; ++s) {
      const bf8 fp = asbf(pack8(ep, s)), fm = asbf(pack8(em, s));
      num = MFMA(ld128(L + K2_KV + s * 1024 + l16), fp, num);
      num = MFMA(ld128(L + K2_KV + 2048 + s * 1024 + l16), fm, num);
      den = MFMA(ld128(L + K2_KV + 4096 + s * 1024 + h * 512), fp, den);
      den = MFMA(ld128(L + K2_KV + 4096 + s * 1024 + h * 512 + 256), fm, den);
    }
    num *= __builtin_amdgcn_rcpf(den[0] + 1e-6f * sp);
    char* od = ws + WS_O + (size_t)((64 + tb) * 32 + head) * 2048 + l16;
    *(u4*)od = pack8(num, 0); *(u4*)(od + 1024) = pack8(num, 1);
  }
  int sidx = 0xDDDD; PIN(sidx);
  const unsigned one2 = 0x3f803f80u, sa = (lane & 4) ? 0u : one2, sb_ = (lane & 4) ? one2 : 0u;
  bf8 sel = asbf((u4){sa, sa, sb_, sb_}); PIN(sel);
  const lptr kvb = L + l16;
  f16v o = f16v{}, sA, sB, negm; f4 lsum = f4{};
  bf8 kA, kB, vA, vB;
#define KVR(i, j) ld128(kvb + ((i) & 15) * 4096 + (j) * 1024)
  kA = KVR(0, 0); kB = KVR(0, 1);
  sA = MFMA(kA, qa[0], f16v{}); sA = MFMA(kB, qa[1], sA);
  const float m0 = xhalf_max(max16(sA));
  negm = splat16(-m0); PIN(negm);
  sA = sA - m0;
  kA = KVR(1, 0); kB = KVR(1, 1); vA = KVR(0, 2); vB = vA;
  sB = f16v{};
#define EX(X, i) X[i] = __builtin_amdgcn_exp2f(X[i])
#define STEP(C, P, i, FIRST, HASK, HASK2, HASV) do { \
    if (!(FIRST)) { o = SMF32(vB, hi8(P), o); } SB(); vB = KVR(i, 3); EX(C, 0); EX(C, 1); EX(C, 2); PIN(C); SB(); \
    if (!(FIRST)) { lsum = SMF16(sel, hi8(P), lsum); } SB(); EX(C, 3); EX(C, 4); PIN(C); SB(); \
    if (HASK) { P = MFMA(kA, qa[0], negm); } SB(); if (HASK2) kA = KVR((i) + 2, 0); EX(C, 5); EX(C, 6); EX(C, 7); PIN(C); SB(); \
    if (HASK) { P = MFMA(kB, qa[1], P); } SB(); if (HASK2) kB = KVR((i) + 2, 1); EX(C, 8); EX(C, 9); EX(C, 10); PIN(C); SB(); \
    o = SMF32(vA, lo8(C), o); SB(); if (HASV) vA = KVR((i) + 1, 2); EX(C, 11); EX(C, 12); EX(C, 13); PIN(C); SB(); \
    lsum = SMF16(sel, lo8(C), lsum); SB(); EX(C, 14); EX(C, 15); PIN(C); SB(); } while (0)
  STEP(sA, sB, 0, true, true, true, true); STEP(sB, sA, 1, false, true, true, true);
  WAIT_BAR(0); DMAT(3);
  STEP(sA, sB, 2, false, true, true, true); STEP(sB, sA, 3, false, true, true, true);
  for (int T = 1; T < 13; T += 4) {
#pragma unroll
    for (int u = 0; u < 4; ++u) {
      const int i0 = 4 * (1 + u);
      STEP(sA, sB, i0, false, true, true, true); STEP(sB, sA, i0 + 1, false, true, true, true);
      WAIT_BAR(2); DMAT(T + u + 3);
      STEP(sA, sB, i0 + 2, false, true, true, true); STEP(sB, sA, i0 + 3, false, true, true, true);
    }
  }
  STEP(sA, sB, 52, false, true, true, true); STEP(sB, sA, 53, false, true, true, true);
  WAIT_BAR(2);
  STEP(sA, sB, 54, false, true, true, true); STEP(sB, sA, 55, false, true, true, true);
  STEP(sA, sB, 56, false, true, true, true); STEP(sB, sA, 57, false, true, true, true);
  WAIT_BAR(0);
  STEP(sA, sB, 58, false, true, true, true); STEP(sB, sA, 59, false, true, true, true);
  STEP(sA, sB, 60, false, true, true, true); STEP(sB, sA, 61, false, true, true, true);
  STEP(sA, sB, 62, false, true, false, true); STEP(sB, sA, 63, false, false, false, false);
  o = SMF32(vB, hi8(sB), o); lsum = SMF16(sel, hi8(sB), lsum);
  float l = lsum[0];
  bool bad = !(l <= 1.8446744e19f);
#pragma unroll
  for (int i = 0; i < 16; ++i) bad |= !(__builtin_fabsf(o[i]) <= 3.0e38f);
  if (__builtin_expect(__any(bad), 0)) {
    float m = -__builtin_inff();
    for (int i = 0; i < 64; ++i) {
      const char* a = KV + i * 4096 + l16;
      f16v s = MFMA(*(const bf8*)a, qa[0], f16v{}); s = MFMA(*(const bf8*)(a + 1024), qa[1], s);
      m = __builtin_fmaxf(m, max16(s));
    }
    m = xhalf_max(m);
    negm = splat16(-m); o = f16v{}; lsum = f4{};
    for (int i = 0; i < 64; ++i) {
      const char* a = KV + i * 4096 + l16;
      f16v s = MFMA(*(const bf8*)a, qa[0], negm); s = MFMA(*(const bf8*)(a + 1024), qa[1], s);
#pragma unroll
      for (int j = 0; j < 16; ++j) EX(s, j);
      o = SMF32(*(const bf8*)(a + 2048), lo8(s), o); o = SMF32(*(const bf8*)(a + 3072), hi8(s), o);
      lsum = SMF16(sel, lo8(s), lsum); lsum = SMF16(sel, hi8(s), lsum);
    }
    l = lsum[0];
  }
  o *= __builtin_amdgcn_rcpf(l);
  char* od = ws + WS_O + (size_t)(tb * 32 + head) * 2048 + l16;
  *(u4*)od = pack8(o, 0); *(u4*)(od + 1024) = pack8(o, 1);
#undef DMAT
#undef KVR
#undef EX
#undef STEP
}

constexpr int K3_LDS = 131072;
struct Frag3 { bf8 x0, x1; s4 w[2][2]; };

__global__ __launch_bounds__(256, 1) void k3_out(const char* __restrict__ woa, const char* __restrict__ wob, const char* __restrict__ ws, char* __restrict__ out) {
  __shared__ __attribute__((aligned(1024))) char smem[K3_LDS];
  const lptr L = (lptr)smem;
  const unsigned L0 = (unsigned)(uintptr_t)L;
  const int tid = threadIdx.x, lane = tid & 63, w = __builtin_amdgcn_readfirstlane(tid >> 6);
  const int bid = blockIdx.x, xcd = bid & 7, br = xcd >> 2, mb = (xcd & 3) * 4 + ((bid >> 3) & 3), nb = bid >> 5;
  const char* O = ws + WS_O + (size_t)(br * 64 + mb * 4 + w) * 65536;
  const char* W = (br ? wob : woa) + nb * 256 + (size_t)w * 16 * 2048;
  const unsigned l16 = lane * 16;
  const unsigned woff = (lane >> 4) * 2048 + (((lane & 15) ^ ((lane >> 4) << 2)) << 4);
#define DMAO(t, i) glds16(O + (t) * 4096 + (i) * 1024, l16, L0 + ((t) & 3) * 32768 + w * 4096 + (i) * 1024)
#define DMAW(t, i) glds16(W + (size_t)(t) * (64 * 2048) + (i) * 8192, woff, L0 + ((t) & 3) * 32768 + 16384 + w * 4096 + (i) * 1024)
#define DMA_ALL(t) do { DMAO(t, 0); DMAW(t, 0); DMAO(t, 1); DMAW(t, 1); DMAO(t, 2); DMAW(t, 2); DMAO(t, 3); DMAW(t, 3); } while (0)
  DMA_ALL(0); DMA_ALL(1); DMA_ALL(2); DMA_ALL(3);
  const int wm = w & 1, wn = w >> 1, r = lane & 31, h = lane >> 5, g1 = (lane >> 4) & 1, q = (lane >> 2) & 3, p = lane & 3;
  lptr xa_[2], wa_[2][2];
#pragma unroll
  for (int half = 0; half < 2; ++half) {
    xa_[half] = L + half * 65536 + wm * 8192 + l16;
#pragma unroll
    for (int nt = 0; nt < 2; ++nt) wa_[half][nt] = L + half * 65536 + 16384 + (4 * h + q) * 256 + (((wn * 8 + nt * 4 + 2 * g1 + (p >> 1)) ^ (q << 2)) << 4) + 8 * (p & 1);
  }
  f16v acc[2][2];
#pragma unroll
  for (int i = 0; i < 2; ++i)
#pragma unroll
    for (int j = 0; j < 2; ++j) acc[i][j] = f16v{};
  Frag3 FA, FB;
#define RX(F, m, slot, ks) F.x##m = ld128(xa_[(slot) >> 1] + ((slot) & 1) * 32768 + (m) * 4096 + (ks) * 1024)
#define RW(F, nt, u, slot, ks) F.w[nt][u] = ldtr(wa_[(slot) >> 1][nt] + ((slot) & 1) * 32768 + (ks) * 4096 + (u) * 2048)
#define WF(F, nt) cat(F.w[nt][0], F.w[nt][1])
#define KSTEP(C, N, RD, slot, ks, D0, D1, D2, D3) do { \
    if (RD) { RW(N, 0, 0, slot, ks); RW(N, 0, 1, slot, ks); } D0; SB(); \
    acc[0][0] = MFMA(WF(C, 0), C.x0, acc[0][0]); SB(); if (RD) { RX(N, 0, slot, ks); RX(N, 1, slot, ks); } D1; SB(); \
    acc[1][0] = MFMA(WF(C, 0), C.x1, acc[1][0]); SB(); if (RD) { RW(N, 1, 0, slot, ks); RW(N, 1, 1, slot, ks); } D2; SB(); \
    acc[0][1] = MFMA(WF(C, 1), C.x0, acc[0][1]); SB(); D3; SB(); \
    acc[1][1] = MFMA(WF(C, 1), C.x1, acc[1][1]); SB(); } while (0)
#define D2_(t, i) do { DMAO(t, i); DMAW(t, i); } while (0)
#define STAGE(t, slot, WB, HASNEXT, HASDMA) do { \
    KSTEP(FA, FB, true, slot, 1, NOP_, NOP_, NOP_, NOP_); \
    KSTEP(FB, FA, true, slot, 2, NOP_, NOP_, NOP_, NOP_); \
    KSTEP(FA, FB, true, slot, 3, NOP_, NOP_, NOP_, NOP_); \
    WB; \
    if (HASDMA) KSTEP(FB, FA, true, ((slot) + 1) & 3, 0, D2_((t) + 4, 0), D2_((t) + 4, 1), D2_((t) + 4, 2), D2_((t) + 4, 3)); \
    else KSTEP(FB, FA, HASNEXT, ((slot) + 1) & 3, 0, NOP_, NOP_, NOP_, NOP_); } while (0)
  WAIT_BAR(24);
  RW(FA, 0, 0, 0, 0); RW(FA, 0, 1, 0, 0); RX(FA, 0, 0, 0); RX(FA, 1, 0, 0); RW(FA, 1, 0, 0, 0); RW(FA, 1, 1, 0, 0);
  for (int t = 0; t < 12; t += 4) {
    STAGE(t, 0, WAIT_BAR(16), true, true); STAGE(t + 1, 1, WAIT_BAR(16), true, true);
    STAGE(t + 2, 2, WAIT_BAR(16), true, true); STAGE(t + 3, 3, WAIT_BAR(16), true, true);
  }
  STAGE(12, 0, WAIT_BAR(16), true, false); STAGE(13, 1, WAIT_BAR(8), true, false);
  STAGE(14, 2, WAIT_BAR(0), true, false); STAGE(15, 3, NOP_, false, false);
  const lptr st = L + w * 8192;
#pragma unroll
  for (int mt = 0; mt < 2; ++mt)
#pragma unroll
    for (int nt = 0; nt < 2; ++nt)
#pragma unroll
      for (int g = 0; g < 4; ++g)
        *(LDSP u2*)(st + (mt * 32 + r) * 128 + (((nt * 4 + g) ^ (r & 7)) << 4) + h * 8) = (u2){pk2(acc[mt][nt][4 * g], acc[mt][nt][4 * g + 1]), pk2(acc[mt][nt][4 * g + 2], acc[mt][nt][4 * g + 3])};
  asm volatile("s_waitcnt lgkmcnt(0)" ::: "memory");
  char* od = out + (size_t)(mb * 128 + wm * 64) * 4096 + br * 2048 + nb * 256 + wn * 128;
#pragma unroll
  for (int i = 0; i < 8; ++i) {
    const int row = i * 8 + (lane >> 3), ch = lane & 7;
    *(u4*)(od + (size_t)row * 4096 + ch * 16) = *(LDSP u4*)(st + row * 128 + ((ch ^ (row & 7)) << 4));
  }
}

extern "C" void kernel_launch(void* const* d_in, const int* in_sizes, int n_in, void* d_out, int out_size, void* d_ws, size_t ws_size, hipStream_t stream) {
  const char* xa = (const char*)d_in[0]; const char* wa = (const char*)d_in[1]; const char* woa = (const char*)d_in[2];
  const char* xb = (const char*)d_in[3]; const char* wb = (const char*)d_in[4]; const char* wphi = (const char*)d_in[5]; const char* wob = (const char*)d_in[6];
  char* ws = (char*)d_ws;
  k1_qkv<<<256, 512, 0, stream>>>(xa, wa, xb, wb, wphi, ws);
  k2_attn<<<256, 512, 0, stream>>>(wphi, ws);
  k3_out<<<256, 256, 0, stream>>>(woa, wob, ws, (char*)d_out);
}
```

```cpp
#include <hip/hip_runtime.h>
#include <hip/hip_bf16.h>
#include <stdint.h>

#define DI __device__ __forceinline__
#define LDSP __attribute__((address_space(3)))
typedef __bf16 bf2 __attribute__((ext_vector_type(2)));
typedef __bf16 bf8 __attribute__((ext_vector_type(8)));
typedef __bf16 bf16v __attribute__((ext_vector_type(16)));
typedef float f2 __attribute__((ext_vector_type(2)));
typedef float f4 __attribute__((ext_vector_type(4)));
typedef float f8 __attribute__((ext_vector_type(8)));
typedef float f16v __attribute__((ext_vector_type(16)));
typedef unsigned u2 __attribute__((ext_vector_type(2)));
typedef unsigned u4 __attribute__((ext_vector_type(4)));
typedef short s4 __attribute__((ext_vector_type(4)));
typedef short s8 __attribute__((ext_vector_type(8)));
typedef LDSP char* lptr;

#define SB() __builtin_amdgcn_sched_barrier(0)
#define PIN(x) asm volatile("" : "+v"(x))
#define MFMA(a, b, c) __builtin_amdgcn_mfma_f32_32x32x16_bf16((a), (b), (c), 0, 0, 0)
#define WAIT_BAR(N) asm volatile("s_waitcnt vmcnt(" #N ") lgkmcnt(0)\n\ts_barrier" ::: "memory")

#define STAMP(i)
#define DIAG_BEGIN
#define DIAG_END(kid, wsp)

constexpr float LOG2E = 1.4426950408889634f;
constexpr float QSCALE = 0.17677669529663687f * 1.4426950408889634f;

constexpr size_t WS_QA = 0;
constexpr size_t WS_KVA = WS_QA + (4u << 20);
constexpr size_t WS_QB = WS_KVA + (8u << 20);
constexpr size_t WS_KVP = WS_QB + (4u << 20);
constexpr size_t WS_O = WS_KVP + (3u << 20);

DI unsigned pk2(float a, float b) { bf2 r = __builtin_convertvector((f2){a, b}, bf2); return __builtin_bit_cast(unsigned, r); }
DI u4 pack8(const f16v& x, int s) { return (u4){pk2(x[8 * s], x[8 * s + 1]), pk2(x[8 * s + 2], x[8 * s + 3]), pk2(x[8 * s + 4], x[8 * s + 5]), pk2(x[8 * s + 6], x[8 * s + 7])}; }
DI bf8 asbf(u4 v) { return __builtin_bit_cast(bf8, v); }
DI bf8 ld128(lptr p) { return *(const LDSP bf8*)p; }
DI s4 ldtr(lptr p) { return __builtin_amdgcn_ds_read_tr16_b64_v4i16((LDSP s4*)p); }
DI bf8 cat(s4 lo, s4 hi) { return __builtin_bit_cast(bf8, __builtin_shufflevector(lo, hi, 0, 1, 2, 3, 4, 5, 6, 7)); }
DI void glds16(const void* sbase, unsigned voff, unsigned lds) {
  asm volatile("s_mov_b32 m0, %2\n\ts_nop 0\n\tglobal_load_lds_dwordx4 %0, %1" ::"v"(voff), "s"(sbase), "s"(lds) : "memory");
}
DI float xhalf_max(float m) { auto rr = __builtin_amdgcn_permlane32_swap(__float_as_uint(m), __float_as_uint(m), false, false); return __builtin_fmaxf(__uint_as_float(rr[0]), __uint_as_float(rr[1])); }
DI float xhalf_min(float m) { auto rr = __builtin_amdgcn_permlane32_swap(__float_as_uint(m), __float_as_uint(m), false, false); return __builtin_fminf(__uint_as_float(rr[0]), __uint_as_float(rr[1])); }
DI float xhalf_sum(float m) { auto rr = __builtin_amdgcn_permlane32_swap(__float_as_uint(m), __float_as_uint(m), false, false); return __uint_as_float(rr[0]) + __uint_as_float(rr[1]); }
DI float max16(const f16v& p) {
  float a = __builtin_fmaxf(__builtin_fmaxf(p[0], p[1]), p[2]), b = __builtin_fmaxf(__builtin_fmaxf(p[3], p[4]), p[5]);
  a = __builtin_fmaxf(__builtin_fmaxf(a, p[6]), p[7]); b = __builtin_fmaxf(__builtin_fmaxf(b, p[8]), p[9]);
  a = __builtin_fmaxf(__builtin_fmaxf(a, p[10]), p[11]); b = __builtin_fmaxf(__builtin_fmaxf(b, p[12]), p[13]);
  a = __builtin_fmaxf(__builtin_fmaxf(a, p[14]), p[15]); return __builtin_fmaxf(a, b);
}
DI float min16(const f16v& p) {
  float a = __builtin_fminf(__builtin_fminf(p[0], p[1]), p[2]), b = __builtin_fminf(__builtin_fminf(p[3], p[4]), p[5]);
  a = __builtin_fminf(__builtin_fminf(a, p[6]), p[7]); b = __builtin_fminf(__builtin_fminf(b, p[8]), p[9]);
  a = __builtin_fminf(__builtin_fminf(a, p[10]), p[11]); b = __builtin_fminf(__builtin_fminf(b, p[12]), p[13]);
  a = __builtin_fminf(__builtin_fminf(a, p[14]), p[15]); return __builtin_fminf(a, b);
}
DI float sum16(const f16v& p) {
  float a = (p[0] + p[1]) + (p[2] + p[3]), b = (p[4] + p[5]) + (p[6] + p[7]), c = (p[8] + p[9]) + (p[10] + p[11]), d = (p[12] + p[13]) + (p[14] + p[15]);
  return (a + b) + (c + d);
}
DI void dual_exp(const f16v& p, f16v& ep, f16v& em, float& sp, float& sm) {
  const float mx = xhalf_max(max16(p)) * LOG2E, mn = xhalf_min(min16(p)) * LOG2E;
#pragma unroll
  for (int r = 0; r < 16; ++r) { ep[r] = __builtin_amdgcn_exp2f(__builtin_fmaf(p[r], LOG2E, -mx)); em[r] = __builtin_amdgcn_exp2f(__builtin_fmaf(p[r], -LOG2E, mn)); }
  sp = xhalf_sum(sum16(ep)); sm = xhalf_sum(sum16(em));
}

constexpr int K1_W = 65536, K1_PHI = 114688, K1_LDS = 118784;
struct Frag1 { bf8 x0, x1; s4 w[3][2]; };

__global__ __launch_bounds__(512, 2) void k1_qkv(const char* __restrict__ xa, const char* __restrict__ wa, const char* __restrict__ xb, const char* __restrict__ wb,
                                                 const char* __restrict__ wphi, char* __restrict__ ws) {
  __shared__ __attribute__((aligned(1024))) char smem[K1_LDS];
  DIAG_BEGIN;
  const lptr L = (lptr)smem;
  const unsigned L0 = (unsigned)(uintptr_t)L;
  const int tid = threadIdx.x, lane = tid & 63, w = __builtin_amdgcn_readfirstlane(tid >> 6);
  const int bid = blockIdx.x, xcd = bid & 7, br = xcd >> 2, hp = (xcd & 3) * 4 + ((bid >> 3) & 3), rb = bid >> 5;
  const char* X = (br ? xb : xa) + (size_t)rb * 256 * 2048;
  const char* W = (br ? wb : wa) + hp * 128;
  unsigned xoff[4], woff[3];
#pragma unroll
  for (int i = 0; i < 4; ++i) { const int row = 32 * w + 8 * i + (lane >> 3); xoff[i] = row * 2048 + (((lane & 7) ^ ((row >> 1) & 7)) << 4); }
#pragma unroll
  for (int i = 0; i < 3; ++i) { const int k = 8 * w + (lane >> 3); woff[i] = k * 6144 + i * 2048 + (((lane & 7) ^ (((k >> 1) & 1) << 2)) << 4); }
#define DMAX(t, slot, i) glds16(X + (t) * 128, xoff[i], L0 + (slot) * 32768 + (4 * w + (i)) * 1024)
#define DMAW(t, slot, i) glds16(W + (size_t)(t) * (64 * 6144), woff[i], L0 + K1_W + (slot) * 24576 + (i) * 8192 + w * 1024)
#define DMA_ALL(t, slot) do { DMAX(t, slot, 0); DMAW(t, slot, 0); DMAX(t, slot, 1); DMAW(t, slot, 1); DMAX(t, slot, 2); DMAW(t, slot, 2); DMAX(t, slot, 3); } while (0)
  DMA_ALL(0, 0);
  if (br && w < 4) glds16(wphi + hp * 4096 + w * 1024, lane * 16, L0 + K1_PHI + w * 1024);
  DMA_ALL(1, 1);

  const int wm = w & 3, hh = w >> 2, r = lane & 31, h = lane >> 5, g1 = (lane >> 4) & 1, q = (lane >> 2) & 3, p = lane & 3;
  lptr xa_[4];
#pragma unroll
  for (int ks = 0; ks < 4; ++ks) xa_[ks] = L + (wm * 64 + r) * 128 + (((h ^ ((r >> 1) & 7)) ^ (2 * ks)) << 4);
  const lptr wa_ = L + K1_W + (8 * h + q) * 128 + (((hh * 4 + 2 * g1 + (p >> 1)) ^ (((q >> 1) & 1) << 2)) << 4) + 8 * (p & 1);

  f16v acc[2][3];
#pragma unroll
  for (int i = 0; i < 2; ++i)
#pragma unroll
    for (int j = 0; j < 3; ++j) acc[i][j] = f16v{};
  Frag1 FA, FB;
#define RX(F, m, slot, ks) F.x##m = ld128(xa_[ks] + (slot) * 32768 + (m) * 4096)
#define RW(F, sg, u, slot, ks) F.w[sg][u] = ldtr(wa_ + (slot) * 24576 + (sg) * 8192 + (ks) * 2048 + (u) * 512)
#define WF(F, sg) cat(F.w[sg][0], F.w[sg][1])
#define KSTEP(C, N, RD, slot, ks, D0, D1, D2, D3, D4, D5, D6) do { \
    if (RD) { RW(N, 0, 0, slot, ks); RW(N, 0, 1, slot, ks); } D0; SB(); \
    acc[0][0] = MFMA(WF(C, 0), C.x0, acc[0][0]); SB(); if (RD) { RX(N, 0, slot, ks); } D1; SB(); \
    acc[1][0] = MFMA(WF(C, 0), C.x1, acc[1][0]); SB(); if (RD) { RX(N, 1, slot, ks); } D2; SB(); \
    acc[0][1] = MFMA(WF(C, 1), C.x0, acc[0][1]); SB(); if (RD) { RW(N, 1, 0, slot, ks); RW(N, 1, 1, slot, ks); } D3; SB(); \
    acc[1][1] = MFMA(WF(C, 1), C.x1, acc[1][1]); SB(); if (RD) { RW(N, 2, 0, slot, ks); RW(N, 2, 1, slot, ks); } D4; SB(); \
    acc[0][2] = MFMA(C.x0, WF(C, 2), acc[0][2]); SB(); D5; SB(); \
    acc[1][2] = MFMA(C.x1, WF(C, 2), acc[1][2]); SB(); D6; SB(); } while (0)
#define NOP_ (void)0
#define STAGE(t, slot, HASNEXT, HASDMA) do { \
    KSTEP(FA, FB, true, slot, 1, NOP_, NOP_, NOP_, NOP_, NOP_, NOP_, NOP_); \
    KSTEP(FB, FA, true, slot, 2, NOP_, NOP_, NOP_, NOP_, NOP_, NOP_, NOP_); \
    KSTEP(FA, FB, true, slot, 3, NOP_, NOP_, NOP_, NOP_, NOP_, NOP_, NOP_); \
    if (HASNEXT) WAIT_BAR(0); \
    if (HASDMA) KSTEP(FB, FA, true, (slot) ^ 1, 0, DMAX((t) + 2, slot, 0), DMAW((t) + 2, slot, 0), DMAX((t) + 2, slot, 1), DMAW((t) + 2, slot, 1), DMAX((t) + 2, slot, 2), DMAW((t) + 2, slot, 2), DMAX((t) + 2, slot, 3)); \
    else KSTEP(FB, FA, HASNEXT, (slot) ^ 1, 0, NOP_, NOP_, NOP_, NOP_, NOP_, NOP_, NOP_); } while (0)
  WAIT_BAR(7);
  STAMP(1);
  RW(FA, 0, 0, 0, 0); RW(FA, 0, 1, 0, 0); RX(FA, 0, 0, 0); RX(FA, 1, 0, 0); RW(FA, 1, 0, 0, 0); RW(FA, 1, 1, 0, 0); RW(FA, 2, 0, 0, 0); RW(FA, 2, 1, 0, 0);
  for (int t = 0; t < 14; t += 2) { STAGE(t, 0, true, true); STAGE(t + 1, 1, true, true); }
  STAMP(2);
  STAGE(14, 0, true, false);
  STAGE(15, 1, false, false);
  STAMP(3);

  const int head = hp * 2 + hh, tb0 = rb * 8 + wm * 2;
  if (br == 0) {
    const int kl = (lane & 0x33) | ((lane & 4) << 1) | ((lane & 8) >> 1);
#pragma unroll
    for (int mt = 0; mt < 2; ++mt) {
      char* qd = ws + WS_QA + (size_t)(head * 64 + tb0 + mt) * 2048;
      char* kd = ws + WS_KVA + (size_t)(head * 64 + tb0 + mt) * 4096;
      const f16v qs = acc[mt][0] * QSCALE;
#pragma unroll
      for (int s = 0; s < 2; ++s) {
        *(u4*)(qd + s * 1024 + lane * 16) = pack8(qs, s);
        *(u4*)(kd + s * 1024 + kl * 16) = pack8(acc[mt][1], s);
        *(u4*)(kd + 2048 + s * 1024 + lane * 16) = pack8(acc[mt][2], s);
      }
    }
  } else {
#pragma unroll
    for (int mt = 0; mt < 2; ++mt) {
      char* qd = ws + WS_QB + (size_t)(head * 64 + tb0 + mt) * 2048;
#pragma unroll
      for (int s = 0; s < 2; ++s) *(u4*)(qd + s * 1024 + lane * 16) = pack8(acc[mt][0], s);
    }
    bf8 wf[2];
#pragma unroll
    for (int s = 0; s < 2; ++s) { const lptr a = L + K1_PHI + hh * 2048 + (16 * s + 4 * h + q) * 64 + g1 * 32 + p * 8; wf[s] = cat(ldtr(a), ldtr(a + 512)); }
    const unsigned one2 = 0x3f803f80u;
    const unsigned op = r < 16 ? one2 : 0u, om = r < 16 ? 0u : one2;
    const bf8 onesP = asbf((u4){op, op, op, op}), onesM = asbf((u4){om, om, om, om});
    f16v kvp = f16v{}, kvm = f16v{}, zz = f16v{};
    const lptr scr = L + w * 4096;
    const lptr wr = scr + r * 64;
    const int fsw = (r >> 1) & 7;
    const lptr rd0 = scr + (4 * h + q) * 64 + (((4 * g1 + p) ^ ((2 * h + (q >> 1)) & 7)) << 3);
    const lptr rd1 = scr + (8 + 4 * h + q) * 64 + (((4 * g1 + p) ^ ((4 + 2 * h + (q >> 1)) & 7)) << 3);
#pragma unroll
    for (int mt = 0; mt < 2; ++mt) {
      f16v pk = MFMA(wf[0], asbf(pack8(acc[mt][1], 0)), f16v{});
      pk = MFMA(wf[1], asbf(pack8(acc[mt][1], 1)), pk);
      f16v ep, em; float sp, sm;
      dual_exp(pk, ep, em, sp, sm);
      ep *= __builtin_amdgcn_rcpf(sp); em *= __builtin_amdgcn_rcpf(sm);
#pragma unroll
      for (int g = 0; g < 4; ++g) {
        *(LDSP u2*)(wr + (((2 * g + h) ^ fsw) << 3)) = (u2){pk2(ep[4 * g], ep[4 * g + 1]), pk2(ep[4 * g + 2], ep[4 * g + 3])};
        *(LDSP u2*)(wr + 2048 + (((2 * g + h) ^ fsw) << 3)) = (u2){pk2(em[4 * g], em[4 * g + 1]), pk2(em[4 * g + 2], em[4 * g + 3])};
      }
      asm volatile("s_waitcnt lgkmcnt(0)" ::: "memory");
#pragma unroll
      for (int s = 0; s < 2; ++s) {
        const bf8 vf = asbf(pack8(acc[mt][2], s));
        const bf8 fp = cat(ldtr(rd0 + s * 1024), ldtr(rd1 + s * 1024)), fm = cat(ldtr(rd0 + 2048 + s * 1024), ldtr(rd1 + 2048 + s * 1024));
        kvp = MFMA(fp, vf, kvp); kvm = MFMA(fm, vf, kvm);
        zz = MFMA(fp, onesP, zz); zz = MFMA(fm, onesM, zz);
      }
      asm volatile("s_waitcnt lgkmcnt(0)" ::: "memory");
    }
    __syncthreads();
    const lptr red = L + w * 12288 + lane * 16;
#pragma unroll
    for (int g = 0; g < 4; ++g) {
      *(LDSP f4*)(red + g * 1024) = (f4){kvp[4 * g], kvp[4 * g + 1], kvp[4 * g + 2], kvp[4 * g + 3]};
      *(LDSP f4*)(red + 4096 + g * 1024) = (f4){kvm[4 * g], kvm[4 * g + 1], kvm[4 * g + 2], kvm[4 * g + 3]};
      *(LDSP f4*)(red + 8192 + g * 1024) = (f4){zz[4 * g], zz[4 * g + 1], zz[4 * g + 2], zz[4 * g + 3]};
    }
    __syncthreads();
    char* pd = ws + WS_KVP + (size_t)(head * 8 + rb) * 12288 + lane * 16;
#pragma unroll
    for (int i = 0; i < 3; ++i) {
      const int piece = wm * 3 + i;
      const lptr a = L + hh * 49152 + piece * 1024 + lane * 16;
      const f4 v = (*(LDSP f4*)a + *(LDSP f4*)(a + 12288)) + (*(LDSP f4*)(a + 24576) + *(LDSP f4*)(a + 36864));
      *(f4*)(pd + piece * 1024) = v;
    }
  }
  DIAG_END(0, ws);
#undef DMAX
#undef DMAW
#undef DMA_ALL
#undef RX
#undef RW
#undef WF
#undef KSTEP
#undef STAGE
}

constexpr int K2_PHI = 65536, K2_KV = K2_PHI + 2048, K2_LDS = K2_KV + 6144;
#define SMF32(a, b, c) __builtin_amdgcn_smfmac_f32_32x32x32_bf16((a), (b), (c), sidx, 0, 0)
#define SMF16(a, b, c) __builtin_amdgcn_smfmac_f32_16x16x64_bf16((a), (b), (c), sidx, 0, 0)
DI bf16v lo8(const f16v& x) { return __builtin_bit_cast(bf16v, __builtin_shufflevector(x, x, 0, 1, 2, 3, 4, 5, 6, 7)); }
DI bf16v hi8(const f16v& x) { return __builtin_bit_cast(bf16v, __builtin_shufflevector(x, x, 8, 9, 10, 11, 12, 13, 14, 15)); }
DI f16v splat16(float v) { return (f16v){v, v, v, v, v, v, v, v, v, v, v, v, v, v, v, v}; }

__global__ __launch_bounds__(512, 2) void k2_attn(const char* __restrict__ wphi, char* __restrict__ ws) {
  __shared__ __attribute__((aligned(1024))) char smem[K2_LDS];
  DIAG_BEGIN;
  const lptr L = (lptr)smem;
  const unsigned L0 = (unsigned)(uintptr_t)L;
  const int tid = threadIdx.x, lane = tid & 63, w = __builtin_amdgcn_readfirstlane(tid >> 6);
  const int bid = blockIdx.x, head = (bid & 7) * 4 + ((bid >> 3) & 3), rb = bid >> 5, tb = rb * 8 + w;
  const int r = lane & 31, h = lane >> 5, g1 = (lane >> 4) & 1, q = (lane >> 2) & 3, p = lane & 3;
  const char* KV = ws + WS_KVA + (size_t)head * 64 * 4096;
  const unsigned l16 = lane * 16;
#define DMAT(T) do { glds16(KV + (T) * 16384 + w * 2048, l16, L0 + ((T) & 3) * 16384 + w * 2048); glds16(KV + (T) * 16384 + w * 2048 + 1024, l16, L0 + ((T) & 3) * 16384 + w * 2048 + 1024); } while (0)
  if (w < 2) glds16(wphi + head * 2048 + w * 1024, l16, L0 + K2_PHI + w * 1024);
  DMAT(0); DMAT(1); DMAT(2);
  bf8 qa[2], qb[2];
#pragma unroll
  for (int s = 0; s < 2; ++s) {
    qa[s] = *(const bf8*)(ws + WS_QA + (size_t)(head * 64 + tb) * 2048 + s * 1024 + l16);
    qb[s] = *(const bf8*)(ws + WS_QB + (size_t)(head * 64 + tb) * 2048 + s * 1024 + l16);
  }
  if (w < 6) {
    const char* src = ws + WS_KVP + (size_t)head * 8 * 12288 + (w >> 1) * 4096 + (w & 1) * 2048 + l16;
    f4 a = *(const f4*)src, b = *(const f4*)(src + 1024);
#pragma unroll
    for (int i = 1; i < 8; ++i) { a += *(const f4*)(src + i * 12288); b += *(const f4*)(src + i * 12288 + 1024); }
    *(LDSP u4*)(L + K2_KV + w * 1024 + l16) = (u4){pk2(a[0], a[1]), pk2(a[2], a[3]), pk2(b[0], b[1]), pk2(b[2], b[3])};
  }
  WAIT_BAR(0);
  STAMP(1);
  {
    bf8 wf[2];
#pragma unroll
    for (int s = 0; s < 2; ++s) { const lptr a = L + K2_PHI + (16 * s + 4 * h + q) * 64 + g1 * 32 + p * 8; wf[s] = cat(ldtr(a), ldtr(a + 512)); }
    f16v pq = MFMA(wf[0], qb[0], f16v{});
    pq = MFMA(wf[1], qb[1], pq);
    f16v ep, em; float sp, sm;
    dual_exp(pq, ep, em, sp, sm);
    em *= sp * __builtin_amdgcn_rcpf(sm);
    f16v num = f16v{}, den = f16v{};
#pragma unroll
    for (int s = 0; s < 2; ++s) {
      const bf8 fp = asbf(pack8(ep, s)), fm = asbf(pack8(em, s));
      num = MFMA(ld128(L + K2_KV + s * 1024 + l16), fp, num);
      num = MFMA(ld128(L + K2_KV + 2048 + s * 1024 + l16), fm, num);
      den = MFMA(ld128(L + K2_KV + 4096 + s * 1024 + h * 512), fp, den);
      den = MFMA(ld128(L + K2_KV + 4096 + s * 1024 + h * 512 + 256), fm, den);
    }
    num *= __builtin_amdgcn_rcpf(den[0] + 1e-6f * sp);
    char* od = ws + WS_O + (size_t)((64 + tb) * 32 + head) * 2048 + l16;
    *(u4*)od = pack8(num, 0); *(u4*)(od + 1024) = pack8(num, 1);
  }
  STAMP(2);
  int sidx = 0xDDDD; PIN(sidx);
  const unsigned one2 = 0x3f803f80u, sa = (lane & 4) ? 0u : one2, sb_ = (lane & 4) ? one2 : 0u;
  bf8 sel = asbf((u4){sa, sa, sb_, sb_}); PIN(sel);
  const lptr kvb = L + l16;
  f16v o = f16v{}, sA, sB, negm; f4 lsum = f4{};
  bf8 kA, kB, vA, vB;
#define KVR(i, j) ld128(kvb + ((i) & 15) * 4096 + (j) * 1024)
  kA = KVR(0, 0); kB = KVR(0, 1);
  sA = MFMA(kA, qa[0], f16v{}); sA = MFMA(kB, qa[1], sA);
  const float m0 = xhalf_max(max16(sA));
  negm = splat16(-m0); PIN(negm);
  sA = sA - m0;
  kA = KVR(1, 0); kB = KVR(1, 1); vA = KVR(0, 2); vB = vA;
  sB = f16v{};
#define EX(X, i) X[i] = __builtin_amdgcn_exp2f(X[i])
#define STEP(C, P, i, FIRST, HASK, HASK2, HASV) do { \
    if (!(FIRST)) { o = SMF32(vB, hi8(P), o); } SB(); vB = KVR(i, 3); EX(C, 0); EX(C, 1); EX(C, 2); PIN(C); SB(); \
    if (!(FIRST)) { lsum = SMF16(sel, hi8(P), lsum); } SB(); EX(C, 3); EX(C, 4); PIN(C); SB(); \
    if (HASK) { P = MFMA(kA, qa[0], negm); } SB(); if (HASK2) kA = KVR((i) + 2, 0); EX(C, 5); EX(C, 6); EX(C, 7); PIN(C); SB(); \
    if (HASK) { P = MFMA(kB, qa[1], P); } SB(); if (HASK2) kB = KVR((i) + 2, 1); EX(C, 8); EX(C, 9); EX(C, 10); PIN(C); SB(); \
    o = SMF32(vA, lo8(C), o); SB(); if (HASV) vA = KVR((i) + 1, 2); EX(C, 11); EX(C, 12); EX(C, 13); PIN(C); SB(); \
    lsum = SMF16(sel, lo8(C), lsum); SB(); EX(C, 14); EX(C, 15); PIN(C); SB(); } while (0)
  STEP(sA, sB, 0, true, true, true, true); STEP(sB, sA, 1, false, true, true, true);
  WAIT_BAR(0); DMAT(3);
  STEP(sA, sB, 2, false, true, true, true); STEP(sB, sA, 3, false, true, true, true);
  STAMP(3);
  for (int T = 1; T < 13; T += 4) {
#pragma unroll
    for (int u = 0; u < 4; ++u) {
      const int i0 = 4 * (1 + u);
      STEP(sA, sB, i0, false, true, true, true); STEP(sB, sA, i0 + 1, false, true, true, true);
      WAIT_BAR(2); DMAT(T + u + 3);
      STEP(sA, sB, i0 + 2, false, true, true, true); STEP(sB, sA, i0 + 3, false, true, true, true);
    }
  }
  STAMP(4);
  STEP(sA, sB, 52, false, true, true, true); STEP(sB, sA, 53, false, true, true, true);
  WAIT_BAR(2);
  STEP(sA, sB, 54, false, true, true, true); STEP(sB, sA, 55, false, true, true, true);
  STEP(sA, sB, 56, false, true, true, true); STEP(sB, sA, 57, false, true, true, true);
  WAIT_BAR(0);
  STEP(sA, sB, 58, false, true, true, true); STEP(sB, sA, 59, false, true, true, true);
  STEP(sA, sB, 60, false, true, true, true); STEP(sB, sA, 61, false, true, true, true);
  STEP(sA, sB, 62, false, true, false, true); STEP(sB, sA, 63, false, false, false, false);
  o = SMF32(vB, hi8(sB), o); lsum = SMF16(sel, hi8(sB), lsum);
  STAMP(5);
  float l = lsum[0];
  bool bad = !(l <= 1.8446744e19f);
#pragma unroll
  for (int i = 0; i < 16; ++i) bad |= !(__builtin_fabsf(o[i]) <= 3.0e38f);
  if (__builtin_expect(__any(bad), 0)) {
    float m = -__builtin_inff();
    for (int i = 0; i < 64; ++i) {
      const char* a = KV + i * 4096 + l16;
      f16v s = MFMA(*(const bf8*)a, qa[0], f16v{}); s = MFMA(*(const bf8*)(a + 1024), qa[1], s);
      m = __builtin_fmaxf(m, max16(s));
    }
    m = xhalf_max(m);
    negm = splat16(-m); o = f16v{}; lsum = f4{};
    for (int i = 0; i < 64; ++i) {
      const char* a = KV + i * 4096 + l16;
      f16v s = MFMA(*(const bf8*)a, qa[0], negm); s = MFMA(*(const bf8*)(a + 1024), qa[1], s);
#pragma unroll
      for (int j = 0; j < 16; ++j) EX(s, j);
      o = SMF32(*(const bf8*)(a + 2048), lo8(s), o); o = SMF32(*(const bf8*)(a + 3072), hi8(s), o);
      lsum = SMF16(sel, lo8(s), lsum); lsum = SMF16(sel, hi8(s), lsum);
    }
    l = lsum[0];
  }
  o *= __builtin_amdgcn_rcpf(l);
  char* od = ws + WS_O + (size_t)(tb * 32 + head) * 2048 + l16;
  *(u4*)od = pack8(o, 0); *(u4*)(od + 1024) = pack8(o, 1);
  DIAG_END(1, ws);
#undef DMAT
#undef KVR
#undef EX
#undef STEP
}

constexpr int K3_LDS = 131072;
struct Frag3 { bf8 x0, x1; s4 w[2][2]; };

__global__ __launch_bounds__(256, 1) void k3_out(const char* __restrict__ woa, const char* __restrict__ wob, const char* __restrict__ ws, char* __restrict__ out) {
  __shared__ __attribute__((aligned(1024))) char smem[K3_LDS];
  DIAG_BEGIN;
  const lptr L = (lptr)smem;
  const unsigned L0 = (unsigned)(uintptr_t)L;
  const int tid = threadIdx.x, lane = tid & 63, w = __builtin_amdgcn_readfirstlane(tid >> 6);
  const int bid = blockIdx.x, xcd = bid & 7, br = xcd >> 2, mb = (xcd & 3) * 4 + ((bid >> 3) & 3), nb = bid >> 5;
  const char* O = ws + WS_O + (size_t)(br * 64 + mb * 4 + w) * 65536;
  const char* W = (br ? wob : woa) + nb * 256 + (size_t)w * 16 * 2048;
  const unsigned l16 = lane * 16;
  const unsigned woff = (lane >> 4) * 2048 + (((lane & 15) ^ ((lane >> 4) << 2)) << 4);
#define DMAO(t, i) glds16(O + (t) * 4096 + (i) * 1024, l16, L0 + ((t) & 3) * 32768 + w * 4096 + (i) * 1024)
#define DMAW(t, i) glds16(W + (size_t)(t) * (64 * 2048) + (i) * 8192, woff, L0 + ((t) & 3) * 32768 + 16384 + w * 4096 + (i) * 1024)
#define DMA_ALL(t) do { DMAO(t, 0); DMAW(t, 0); DMAO(t, 1); DMAW(t, 1); DMAO(t, 2); DMAW(t, 2); DMAO(t, 3); DMAW(t, 3); } while (0)
  DMA_ALL(0); DMA_ALL(1); DMA_ALL(2); DMA_ALL(3);
  const int wm = w & 1, wn = w >> 1, r = lane & 31, h = lane >> 5, g1 = (lane >> 4) & 1, q = (lane >> 2) & 3, p = lane & 3;
  lptr xa_[2], wa_[2][2];
#pragma unroll
  for (int half = 0; half < 2; ++half) {
    xa_[half] = L + half * 65536 + wm * 8192 + l16;
#pragma unroll
    for (int nt = 0; nt < 2; ++nt) wa_[half][nt] = L + half * 65536 + 16384 + (4 * h + q) * 256 + (((wn * 8 + nt * 4 + 2 * g1 + (p >> 1)) ^ (q << 2)) << 4) + 8 * (p & 1);
  }
  f16v acc[2][2];
#pragma unroll
  for (int i = 0; i < 2; ++i)
#pragma unroll
    for (int j = 0; j < 2; ++j) acc[i][j] = f16v{};
  Frag3 FA, FB;
#define RX(F, m, slot, ks) F.x##m = ld128(xa_[(slot) >> 1] + ((slot) & 1) * 32768 + (m) * 4096 + (ks) * 1024)
#define RW(F, nt, u, slot, ks) F.w[nt][u] = ldtr(wa_[(slot) >> 1][nt] + ((slot) & 1) * 32768 + (ks) * 4096 + (u) * 2048)
#define WF(F, nt) cat(F.w[nt][0], F.w[nt][1])
#define KSTEP(C, N, RD, slot, ks, D0, D1, D2, D3) do { \
    if (RD) { RW(N, 0, 0, slot, ks); RW(N, 0, 1, slot, ks); } D0; SB(); \
    acc[0][0] = MFMA(WF(C, 0), C.x0, acc[0][0]); SB(); if (RD) { RX(N, 0, slot, ks); RX(N, 1, slot, ks); } D1; SB(); \
    acc[1][0] = MFMA(WF(C, 0), C.x1, acc[1][0]); SB(); if (RD) { RW(N, 1, 0, slot, ks); RW(N, 1, 1, slot, ks); } D2; SB(); \
    acc[0][1] = MFMA(WF(C, 1), C.x0, acc[0][1]); SB(); D3; SB(); \
    acc[1][1] = MFMA(WF(C, 1), C.x1, acc[1][1]); SB(); } while (0)
#define D2_(t, i) do { DMAO(t, i); DMAW(t, i); } while (0)
#define STAGE(t, slot, WB, HASNEXT, HASDMA) do { \
    KSTEP(FA, FB, true, slot, 1, NOP_, NOP_, NOP_, NOP_); \
    KSTEP(FB, FA, true, slot, 2, NOP_, NOP_, NOP_, NOP_); \
    KSTEP(FA, FB, true, slot, 3, NOP_, NOP_, NOP_, NOP_); \
    WB; \
    if (HASDMA) KSTEP(FB, FA, true, ((slot) + 1) & 3, 0, D2_((t) + 4, 0), D2_((t) + 4, 1), D2_((t) + 4, 2), D2_((t) + 4, 3)); \
    else KSTEP(FB, FA, HASNEXT, ((slot) + 1) & 3, 0, NOP_, NOP_, NOP_, NOP_); } while (0)
  WAIT_BAR(24);
  STAMP(1);
  RW(FA, 0, 0, 0, 0); RW(FA, 0, 1, 0, 0); RX(FA, 0, 0, 0); RX(FA, 1, 0, 0); RW(FA, 1, 0, 0, 0); RW(FA, 1, 1, 0, 0);
  for (int t = 0; t < 12; t += 4) {
    STAGE(t, 0, WAIT_BAR(16), true, true); STAGE(t + 1, 1, WAIT_BAR(16), true, true);
    STAGE(t + 2, 2, WAIT_BAR(16), true, true); STAGE(t + 3, 3, WAIT_BAR(16), true, true);
  }
  STAMP(2);
  STAGE(12, 0, WAIT_BAR(16), true, false); STAGE(13, 1, WAIT_BAR(8), true, false);
  STAGE(14, 2, WAIT_BAR(0), true, false); STAGE(15, 3, NOP_, false, false);
  STAMP(3);
  const lptr st = L + w * 8192;
#pragma unroll
  for (int mt = 0; mt < 2; ++mt)
#pragma unroll
    for (int nt = 0; nt < 2; ++nt)
#pragma unroll
      for (int g = 0; g < 4; ++g)
        *(LDSP u2*)(st + (mt * 32 + r) * 128 + (((nt * 4 + g) ^ (r & 7)) << 4) + h * 8) = (u2){pk2(acc[mt][nt][4 * g], acc[mt][nt][4 * g + 1]), pk2(acc[mt][nt][4 * g + 2], acc[mt][nt][4 * g + 3])};
  asm volatile("s_waitcnt lgkmcnt(0)" ::: "memory");
  char* od = out + (size_t)(mb * 128 + wm * 64) * 4096 + br * 2048 + nb * 256 + wn * 128;
#pragma unroll
  for (int i = 0; i < 8; ++i) {
    const int row = i * 8 + (lane >> 3), ch = lane & 7;
    *(u4*)(od + (size_t)row * 4096 + ch * 16) = *(LDSP u4*)(st + row * 128 + ((ch ^ (row & 7)) << 4));
  }
  DIAG_END(2, ws);
}

extern "C" void kernel_launch(void* const* d_in, const int* in_sizes, int n_in, void* d_out, int out_size, void* d_ws, size_t ws_size, hipStream_t stream) {
  const char* xa = (const char*)d_in[0]; const char* wa = (const char*)d_in[1]; const char* woa = (const char*)d_in[2];
  const char* xb = (const char*)d_in[3]; const char* wb = (const char*)d_in[4]; const char* wphi = (const char*)d_in[5]; const char* wob = (const char*)d_in[6];
  char* ws = (char*)d_ws;
  k1_qkv<<<256, 512, 0, stream>>>(xa, wa, xb, wb, wphi, ws);
  k2_attn<<<256, 512, 0, stream>>>(wphi, ws);
  k3_out<<<256, 256, 0, stream>>>(woa, wob, ws, (char*)d_out);
}
```

```cpp
#include <hip/hip_runtime.h>
#include <hip/hip_bf16.h>
#include <stdint.h>

#define DI __device__ __forceinline__
#define LDSP __attribute__((address_space(3)))
typedef __bf16 bf2 __attribute__((ext_vector_type(2)));
typedef __bf16 bf8 __attribute__((ext_vector_type(8)));
typedef __bf16 bf16v __attribute__((ext_vector_type(16)));
typedef float f2 __attribute__((ext_vector_type(2)));
typedef float f4 __attribute__((ext_vector_type(4)));
typedef float f8 __attribute__((ext_vector_type(8)));
typedef float f16v __attribute__((ext_vector_type(16)));
typedef unsigned u2 __attribute__((ext_vector_type(2)));
typedef unsigned u4 __attribute__((ext_vector_type(4)));
typedef short s4 __attribute__((ext_vector_type(4)));
typedef short s8 __attribute__((ext_vector_type(8)));
typedef LDSP char* lptr;

#define SB() __builtin_amdgcn_sched_barrier(0)
#define PIN(x) asm volatile("" : "+v"(x))
#define MFMA(a, b, c) __builtin_amdgcn_mfma_f32_32x32x16_bf16((a), (b), (c), 0, 0, 0)
#define WAIT_BARV(N) asm volatile("s_waitcnt vmcnt(" #N ")\n\ts_barrier" ::: "memory")
#define WAIT_BAR(N) asm volatile("s_waitcnt vmcnt(" #N ") lgkmcnt(0)\n\ts_barrier" ::: "memory")

#define STAMP(i)
#define DIAG_BEGIN
#define DIAG_END(kid, wsp)

constexpr float LOG2E = 1.4426950408889634f;
constexpr float QSCALE = 0.17677669529663687f * 1.4426950408889634f;

constexpr size_t WS_QA = 0;
constexpr size_t WS_KVA = WS_QA + (4u << 20);
constexpr size_t WS_QB = WS_KVA + (8u << 20);
constexpr size_t WS_KVP = WS_QB + (4u << 20);
constexpr int KVP_STRIDE = 8448;
constexpr size_t WS_O = WS_KVP + (3u << 20);

DI unsigned pk2(float a, float b) { bf2 r = __builtin_convertvector((f2){a, b}, bf2); return __builtin_bit_cast(unsigned, r); }
DI u4 pack8(const f16v& x, int s) { return (u4){pk2(x[8 * s], x[8 * s + 1]), pk2(x[8 * s + 2], x[8 * s + 3]), pk2(x[8 * s + 4], x[8 * s + 5]), pk2(x[8 * s + 6], x[8 * s + 7])}; }
DI bf8 asbf(u4 v) { return __builtin_bit_cast(bf8, v); }
DI bf8 ld128(lptr p) { return *(const LDSP bf8*)p; }
DI s4 ldtr(lptr p) { return __builtin_amdgcn_ds_read_tr16_b64_v4i16((LDSP s4*)p); }
DI bf8 cat(s4 lo, s4 hi) { return __builtin_bit_cast(bf8, __builtin_shufflevector(lo, hi, 0, 1, 2, 3, 4, 5, 6, 7)); }
DI void glds16(const void* sbase, unsigned voff, unsigned lds) {
  asm volatile("s_mov_b32 m0, %2\n\ts_nop 0\n\tglobal_load_lds_dwordx4 %0, %1" ::"v"(voff), "s"(sbase), "s"(lds) : "memory");
}
DI float xhalf_max(float m) { auto rr = __builtin_amdgcn_permlane32_swap(__float_as_uint(m), __float_as_uint(m), false, false); return __builtin_fmaxf(__uint_as_float(rr[0]), __uint_as_float(rr[1])); }
DI float xhalf_min(float m) { auto rr = __builtin_amdgcn_permlane32_swap(__float_as_uint(m), __float_as_uint(m), false, false); return __builtin_fminf(__uint_as_float(rr[0]), __uint_as_float(rr[1])); }
DI float xhalf_sum(float m) { auto rr = __builtin_amdgcn_permlane32_swap(__float_as_uint(m), __float_as_uint(m), false, false); return __uint_as_float(rr[0]) + __uint_as_float(rr[1]); }
DI float max16(const f16v& p) {
  float a = __builtin_fmaxf(__builtin_fmaxf(p[0], p[1]), p[2]), b = __builtin_fmaxf(__builtin_fmaxf(p[3], p[4]), p[5]);
  a = __builtin_fmaxf(__builtin_fmaxf(a, p[6]), p[7]); b = __builtin_fmaxf(__builtin_fmaxf(b, p[8]), p[9]);
  a = __builtin_fmaxf(__builtin_fmaxf(a, p[10]), p[11]); b = __builtin_fmaxf(__builtin_fmaxf(b, p[12]), p[13]);
  a = __builtin_fmaxf(__builtin_fmaxf(a, p[14]), p[15]); return __builtin_fmaxf(a, b);
}
DI float min16(const f16v& p) {
  float a = __builtin_fminf(__builtin_fminf(p[0], p[1]), p[2]), b = __builtin_fminf(__builtin_fminf(p[3], p[4]), p[5]);
  a = __builtin_fminf(__builtin_fminf(a, p[6]), p[7]); b = __builtin_fminf(__builtin_fminf(b, p[8]), p[9]);
  a = __builtin_fminf(__builtin_fminf(a, p[10]), p[11]); b = __builtin_fminf(__builtin_fminf(b, p[12]), p[13]);
  a = __builtin_fminf(__builtin_fminf(a, p[14]), p[15]); return __builtin_fminf(a, b);
}
DI float sum16(const f16v& p) {
  float a = (p[0] + p[1]) + (p[2] + p[3]), b = (p[4] + p[5]) + (p[6] + p[7]), c = (p[8] + p[9]) + (p[10] + p[11]), d = (p[12] + p[13]) + (p[14] + p[15]);
  return (a + b) + (c + d);
}
DI void dual_exp(const f16v& p, f16v& ep, f16v& em, float& sp, float& sm) {
  const float mx = xhalf_max(max16(p)) * LOG2E, mn = xhalf_min(min16(p)) * LOG2E;
#pragma unroll
  for (int r = 0; r < 16; ++r) { ep[r] = __builtin_amdgcn_exp2f(__builtin_fmaf(p[r], LOG2E, -mx)); em[r] = __builtin_amdgcn_exp2f(__builtin_fmaf(p[r], -LOG2E, mn)); }
  sp = xhalf_sum(sum16(ep)); sm = xhalf_sum(sum16(em));
}

constexpr int K1_W = 98304, K1_PHI = 147456, K1_PF = K1_PHI + 4096, K1_LDS = K1_PF + 256;
struct Frag1 { bf8 x[4]; s4 w[3][2][2]; };
#define MFMA16(a, b, c) __builtin_amdgcn_mfma_f32_16x16x32_bf16((a), (b), (c), 0, 0, 0)
DI u4 pack44(const f4& a, const f4& b) { return (u4){pk2(a[0], a[1]), pk2(a[2], a[3]), pk2(b[0], b[1]), pk2(b[2], b[3])}; }
DI void glds4(const void* sbase, unsigned voff, unsigned lds) {
  asm volatile("s_mov_b32 m0, %2\n\ts_nop 0\n\tglobal_load_lds_dword %0, %1" ::"v"(voff), "s"(sbase), "s"(lds) : "memory");
}
DI float xq_max(float m) { m = xhalf_max(m); auto rr = __builtin_amdgcn_permlane16_swap(__float_as_uint(m), __float_as_uint(m), false, false); return __builtin_fmaxf(__uint_as_float(rr[0]), __uint_as_float(rr[1])); }
DI float xq_min(float m) { m = xhalf_min(m); auto rr = __builtin_amdgcn_permlane16_swap(__float_as_uint(m), __float_as_uint(m), false, false); return __builtin_fminf(__uint_as_float(rr[0]), __uint_as_float(rr[1])); }
DI float xq_sum(float m) { m = xhalf_sum(m); auto rr = __builtin_amdgcn_permlane16_swap(__float_as_uint(m), __float_as_uint(m), false, false); return __uint_as_float(rr[0]) + __uint_as_float(rr[1]); }

__global__ __launch_bounds__(512, 2) void k1_qkv(const char* __restrict__ xa, const char* __restrict__ wa, const char* __restrict__ xb, const char* __restrict__ wb,
                                                 const char* __restrict__ wphi, char* __restrict__ ws) {
  __shared__ __attribute__((aligned(1024))) char smem[K1_LDS];
  DIAG_BEGIN;
  const lptr L = (lptr)smem;
  const unsigned L0 = (unsigned)(uintptr_t)L;
  const int tid = threadIdx.x, lane = tid & 63, w = __builtin_amdgcn_readfirstlane(tid >> 6);
  const int bid = blockIdx.x, xcd = bid & 7, br = xcd >> 2, hp = (xcd & 3) * 4 + ((bid >> 3) & 3), rb = bid >> 5;
  const char* X = (br ? xb : xa) + (size_t)rb * 256 * 2048;
  const char* W = (br ? wb : wa) + hp * 128;
  unsigned xoff[4], woff[3];
#pragma unroll
  for (int i = 0; i < 4; ++i) { const int row = 32 * w + 8 * i + (lane >> 3); xoff[i] = row * 2048 + (((lane & 7) ^ ((row >> 1) & 7)) << 4); }
#pragma unroll
  for (int i = 0; i < 3; ++i) woff[i] = (8 * w + (lane >> 3)) * 6144 + i * 2048 + (((lane & 7) ^ ((((lane >> 4) & 1) | ((w & 1) << 1)) << 1)) << 4);
  const unsigned pfoff = (8 * rb + w) * 6144 + ((lane & 3) == 3 ? 0 : (lane & 3)) * 2048;
#define DMAX(t, xs, i) glds16(X + (t) * 128, xoff[i], L0 + (xs) + (4 * w + (i)) * 1024)
#define DMAW(t, slot, i) glds16(W + (size_t)(t) * (64 * 6144), woff[i], L0 + K1_W + (slot) * 24576 + (i) * 8192 + w * 1024)
#define PFW(t) glds4(W + (size_t)(t) * (64 * 6144), pfoff, L0 + K1_PF)
  DMAW(0, 0, 0); DMAX(0, 0, 0); DMAX(0, 0, 1); DMAW(0, 0, 1); DMAX(0, 0, 2); DMAX(0, 0, 3); DMAW(0, 0, 2);
  PFW(2); PFW(3);
  if (br && w < 4) glds16(wphi + hp * 4096 + w * 1024, lane * 16, L0 + K1_PHI + w * 1024);
  DMAW(1, 1, 0); DMAW(1, 1, 1); DMAW(1, 1, 2);
  DMAX(1, 32768, 0); DMAX(1, 32768, 1); DMAX(1, 32768, 2); DMAX(1, 32768, 3);
  DMAX(2, 65536, 0); DMAX(2, 65536, 1); DMAX(2, 65536, 2); DMAX(2, 65536, 3);
  PFW(4);

  const int wm = w & 3, hh = w >> 2, t16 = lane & 15, g = lane >> 4, q = (lane >> 2) & 3, p = lane & 3;
  lptr xb_[2], wa_[2];
#pragma unroll
  for (int kk = 0; kk < 2; ++kk) xb_[kk] = L + (wm * 64 + t16) * 128 + (((g ^ (t16 >> 1)) ^ (4 * kk)) << 4);
#pragma unroll
  for (int j = 0; j < 2; ++j) wa_[j] = L + K1_W + (8 * g + q) * 128 + (((hh * 2 + j) ^ ((q >> 1) | ((g & 1) << 1))) << 5) + 8 * p;

  f4 acc[4][3][2];
#pragma unroll
  for (int i = 0; i < 4; ++i)
#pragma unroll
    for (int j = 0; j < 6; ++j) acc[i][j >> 1][j & 1] = f4{};
  Frag1 FA, FB;
#define RX(F, i, xp) F.x[i] = ld128((xp) + (i) * 2048)
#define RW(F, sg, j, slot, kk) do { F.w[sg][j][0] = ldtr(wa_[j] + (slot) * 24576 + (sg) * 8192 + (kk) * 4096); F.w[sg][j][1] = ldtr(wa_[j] + (slot) * 24576 + (sg) * 8192 + (kk) * 4096 + 512); } while (0)
#define WF(F, sg, j) cat(F.w[sg][j][0], F.w[sg][j][1])
#define MM(C, i, sg, j) do { if ((sg) < 2) acc[i][sg][j] = MFMA16(WF(C, sg, j), C.x[i], acc[i][sg][j]); else acc[i][sg][j] = MFMA16(C.x[i], WF(C, sg, j), acc[i][sg][j]); SB(); } while (0)
#define G4(C, sg, j, A0, A1, A2, A3) do { MM(C, 0, sg, j); A0; SB(); MM(C, 1, sg, j); A1; SB(); MM(C, 2, sg, j); A2; SB(); MM(C, 3, sg, j); A3; SB(); } while (0)
#define IFR(RD, x) do { if (RD) { x; } } while (0)
#define KSTEP(C, N, RD, xp, slot, kk, D0, D1, D2, D3) do { \
    G4(C, 0, 0, IFR(RD, RW(N, 0, 0, slot, kk)), IFR(RD, RX(N, 0, xp)), IFR(RD, RX(N, 1, xp)), D0); \
    G4(C, 0, 1, IFR(RD, RX(N, 2, xp)), IFR(RD, RX(N, 3, xp)), IFR(RD, RW(N, 0, 1, slot, kk)), NOP_); \
    G4(C, 1, 0, IFR(RD, RW(N, 1, 0, slot, kk)), D1, IFR(RD, RW(N, 1, 1, slot, kk)), NOP_); \
    G4(C, 1, 1, IFR(RD, RW(N, 2, 0, slot, kk)), NOP_, IFR(RD, RW(N, 2, 1, slot, kk)), D2); \
    G4(C, 2, 0, NOP_, NOP_, NOP_, NOP_); \
    G4(C, 2, 1, D3, NOP_, NOP_, NOP_); } while (0)
#define NOP_ (void)0
#define STAGE(t, slot, WB, HASNEXT, DX_PREV, DW, DX) do { \
    const lptr x1_ = xb_[1] + xs; \
    if (DX_PREV) KSTEP(FA, FB, true, x1_, slot, 1, DMAX((t) + 2, xp, 1), DMAX((t) + 2, xp, 2), DMAX((t) + 2, xp, 3), PFW((t) + 4 > 15 ? 15 : (t) + 4)); \
    else KSTEP(FA, FB, true, x1_, slot, 1, NOP_, NOP_, NOP_, NOP_); \
    xp = xs; xs = xs == 65536 ? 0 : xs + 32768; \
    const lptr x0_ = xb_[0] + xs; \
    WB; \
    if (DW) KSTEP(FB, FA, true, x0_, (slot) ^ 1, 0, DMAW((t) + 2, slot, 0), DMAW((t) + 2, slot, 1), DMAW((t) + 2, slot, 2), IFR(DX, DMAX((t) + 3, xp, 0))); \
    else KSTEP(FB, FA, HASNEXT, x0_, (slot) ^ 1, 0, NOP_, NOP_, NOP_, NOP_); } while (0)
  unsigned xs = 0, xp = 0;
  if (br && w < 4) WAIT_BAR(15); else WAIT_BAR(14);
  STAMP(1);
  RW(FA, 0, 0, 0, 0); RX(FA, 0, xb_[0]); RX(FA, 1, xb_[0]); RX(FA, 2, xb_[0]); RX(FA, 3, xb_[0]);
  RW(FA, 0, 1, 0, 0); RW(FA, 1, 0, 0, 0); RW(FA, 1, 1, 0, 0); RW(FA, 2, 0, 0, 0); RW(FA, 2, 1, 0, 0);
  STAGE(0, 0, WAIT_BAR(5), true, false, true, true);
  for (int t = 1; t < 13; t += 2) { STAGE(t, 1, WAIT_BAR(5), true, true, true, true); STAGE(t + 1, 0, WAIT_BAR(5), true, true, true, true); }
  STAGE(13, 1, WAIT_BAR(5), true, true, true, false);
  STAMP(2);
  STAGE(14, 0, WAIT_BAR(0), true, false, false, false);
  STAGE(15, 1, NOP_, false, false, false, false);
  STAMP(3);

  const int head = hp * 2 + hh, tb0 = rb * 8 + wm * 2;
  const int fo = (g >> 1) * 1024 + (g & 1) * 512;
  if (br == 0) {
    const int ks = (t16 >> 2) * 8 + (t16 & 3);
#pragma unroll
    for (int m = 0; m < 2; ++m) {
      char* qd = ws + WS_QA + (size_t)(head * 64 + tb0 + m) * 2048 + fo;
      char* kd = ws + WS_KVA + (size_t)(head * 64 + tb0 + m) * 4096 + fo;
#pragma unroll
      for (int a = 0; a < 2; ++a) {
        *(u4*)(qd + (a * 16 + t16) * 16) = pack44(acc[2 * m + a][0][0] * QSCALE, acc[2 * m + a][0][1] * QSCALE);
        *(u4*)(kd + (ks + 4 * a) * 16) = pack44(acc[2 * m + a][1][0], acc[2 * m + a][1][1]);
        *(u4*)(kd + 2048 + (a * 16 + t16) * 16) = pack44(acc[2 * m][2][a], acc[2 * m + 1][2][a]);
      }
    }
  } else {
#pragma unroll
    for (int i = 0; i < 4; ++i) *(u4*)(ws + WS_QB + (size_t)(head * 64 + tb0 + (i >> 1)) * 2048 + fo + ((i & 1) * 16 + t16) * 16) = pack44(acc[i][0][0], acc[i][0][1]);
    bf8 wf[2];
#pragma unroll
    for (int ft = 0; ft < 2; ++ft) { const lptr a = L + K1_PHI + hh * 2048 + (4 * g + q) * 64 + ft * 32 + p * 8; wf[ft] = cat(ldtr(a), ldtr(a + 1024)); }
    const unsigned one2 = 0x3f803f80u;
    const unsigned op = t16 < 8 ? one2 : 0u, om = t16 < 8 ? 0u : one2;
    const bf8 onesP = asbf((u4){op, op, op, op}), onesM = asbf((u4){om, om, om, om});
    const lptr scr = L + 32768 + w * 8192;
#pragma unroll
    for (int i = 0; i < 4; ++i) {
      const bf8 kf = asbf(pack44(acc[i][1][0], acc[i][1][1]));
      const f4 p0 = MFMA16(wf[0], kf, f4{}), p1 = MFMA16(wf[1], kf, f4{});
      float mx = __builtin_fmaxf(__builtin_fmaxf(__builtin_fmaxf(p0[0], p0[1]), p0[2]), __builtin_fmaxf(__builtin_fmaxf(__builtin_fmaxf(p0[3], p1[0]), p1[1]), __builtin_fmaxf(p1[2], p1[3])));
      float mn = __builtin_fminf(__builtin_fminf(__builtin_fminf(p0[0], p0[1]), p0[2]), __builtin_fminf(__builtin_fminf(__builtin_fminf(p0[3], p1[0]), p1[1]), __builtin_fminf(p1[2], p1[3])));
      mx = xq_max(mx) * LOG2E; mn = xq_min(mn) * LOG2E;
      f4 e[4];
#pragma unroll
      for (int r = 0; r < 4; ++r) {
        e[0][r] = __builtin_amdgcn_exp2f(__builtin_fmaf(p0[r], LOG2E, -mx)); e[1][r] = __builtin_amdgcn_exp2f(__builtin_fmaf(p1[r], LOG2E, -mx));
        e[2][r] = __builtin_amdgcn_exp2f(__builtin_fmaf(p0[r], -LOG2E, mn)); e[3][r] = __builtin_amdgcn_exp2f(__builtin_fmaf(p1[r], -LOG2E, mn));
      }
      const f4 tp = e[0] + e[1], tm = e[2] + e[3];
      const float rp = __builtin_amdgcn_rcpf(xq_sum((tp[0] + tp[1]) + (tp[2] + tp[3]))), rm = __builtin_amdgcn_rcpf(xq_sum((tm[0] + tm[1]) + (tm[2] + tm[3])));
      e[0] *= rp; e[1] *= rp; e[2] *= rm; e[3] *= rm;
#pragma unroll
      for (int c = 0; c < 4; ++c)
        *(LDSP u2*)(scr + (i * 16 + t16) * 128 + ((c ^ ((t16 >> 1) & 3)) << 5) + ((g ^ ((t16 >> 3) << 1)) << 3)) = (u2){pk2(e[c][0], e[c][1]), pk2(e[c][2], e[c][3])};
    }
    asm volatile("s_waitcnt lgkmcnt(0)" ::: "memory");
    f4 kv[4][2], zz[2];
#pragma unroll
    for (int c = 0; c < 4; ++c) { kv[c][0] = f4{}; kv[c][1] = f4{}; }
    zz[0] = f4{}; zz[1] = f4{};
#pragma unroll
    for (int m = 0; m < 2; ++m) {
      const bf8 v0 = asbf(pack44(acc[2 * m][2][0], acc[2 * m + 1][2][0])), v1 = asbf(pack44(acc[2 * m][2][1], acc[2 * m + 1][2][1]));
#pragma unroll
      for (int c = 0; c < 4; ++c) {
        const lptr a = scr + (m * 32 + 4 * g + q) * 128 + ((c ^ (((g & 1) << 1) | (q >> 1))) << 5) + ((p ^ (g & 2)) << 3);
        const bf8 f = cat(ldtr(a), ldtr(a + 2048));
        kv[c][0] = MFMA16(f, v0, kv[c][0]); kv[c][1] = MFMA16(f, v1, kv[c][1]);
        zz[c & 1] = MFMA16(f, c < 2 ? onesP : onesM, zz[c & 1]);
      }
    }
    const lptr red = L + 65536 + 32768 * 0;
    asm volatile("s_waitcnt lgkmcnt(0)\n\ts_barrier" ::: "memory");
#pragma unroll
    for (int c = 0; c < 4; ++c) { *(LDSP f4*)(red + w * 10240 + (c * 2) * 1024 + lane * 16) = kv[c][0]; *(LDSP f4*)(red + w * 10240 + (c * 2 + 1) * 1024 + lane * 16) = kv[c][1]; }
    *(LDSP f4*)(red + w * 10240 + 8192 + lane * 16) = zz[0]; *(LDSP f4*)(red + w * 10240 + 9216 + lane * 16) = zz[1];
    asm volatile("s_waitcnt lgkmcnt(0)\n\ts_barrier" ::: "memory");
    char* pd = ws + WS_KVP + (size_t)(head * 8 + rb) * KVP_STRIDE;
#pragma unroll
    for (int i = 0; i < 3; ++i) {
      const int piece = i < 2 ? wm * 2 + i : 8 + wm;
      if (i < 2 || (wm < 2 && (t16 & 7) == 0)) {
        const lptr a = red + hh * 40960 + piece * 1024 + lane * 16;
        const f4 v = (*(LDSP f4*)a + *(LDSP f4*)(a + 10240)) + (*(LDSP f4*)(a + 20480) + *(LDSP f4*)(a + 30720));
        *(f4*)(pd + (i < 2 ? piece * 1024 + lane * 16 : 8192 + (t16 + wm * 4 + g) * 16)) = v;
      }
    }
  }
  DIAG_END(0, ws);
#undef DMAX
#undef DMAW
#undef PFW
#undef RX
#undef RW
#undef WF
#undef MM
#undef G4
#undef KSTEP
#undef STAGE
}

constexpr int K2_PHI = 65536, K2_KV = K2_PHI + 2048, K2_LDS = K2_KV + 6144;
#define SMF32(a, b, c) __builtin_amdgcn_smfmac_f32_32x32x32_bf16((a), (b), (c), sidx, 0, 0)
#define SMF16(a, b, c) __builtin_amdgcn_smfmac_f32_16x16x64_bf16((a), (b), (c), sidx, 0, 0)
DI bf16v lo8(const f16v& x) { return __builtin_bit_cast(bf16v, __builtin_shufflevector(x, x, 0, 1, 2, 3, 4, 5, 6, 7)); }
DI bf16v hi8(const f16v& x) { return __builtin_bit_cast(bf16v, __builtin_shufflevector(x, x, 8, 9, 10, 11, 12, 13, 14, 15)); }
DI f16v splat16(float v) { return (f16v){v, v, v, v, v, v, v, v, v, v, v, v, v, v, v, v}; }

__global__ __launch_bounds__(512, 2) void k2_attn(const char* __restrict__ wphi, char* __restrict__ ws) {
  __shared__ __attribute__((aligned(1024))) char smem[K2_LDS];
  DIAG_BEGIN;
  const lptr L = (lptr)smem;
  const unsigned L0 = (unsigned)(uintptr_t)L;
  const int tid = threadIdx.x, lane = tid & 63, w = __builtin_amdgcn_readfirstlane(tid >> 6);
  const int bid = blockIdx.x, head = (bid & 7) * 4 + ((bid >> 3) & 3), rb = bid >> 5, tb = rb * 8 + w;
  const int r = lane & 31, h = lane >> 5, g1 = (lane >> 4) & 1, q = (lane >> 2) & 3, p = lane & 3;
  const char* KV = ws + WS_KVA + (size_t)head * 64 * 4096;
  const unsigned l16 = lane * 16;
#define DMAT(T) do { glds16(KV + (T) * 16384 + w * 2048, l16, L0 + ((T) & 3) * 16384 + w * 2048); glds16(KV + (T) * 16384 + w * 2048 + 1024, l16, L0 + ((T) & 3) * 16384 + w * 2048 + 1024); } while (0)
#define DMAH(T, o) glds16(KV + (T) * 16384 + w * 2048 + (o), l16, L0 + ((T) & 3) * 16384 + w * 2048 + (o))
  DMAT(0);
  bf8 qa[2];
  {
    const char* qs = ws + WS_QA + (size_t)(head * 64 + tb) * 2048;
    asm volatile("global_load_dwordx4 %0, %2, %3\n\tglobal_load_dwordx4 %1, %2, %3 offset:1024" : "=&v"(qa[0]), "=&v"(qa[1]) : "v"(l16), "s"(qs) : "memory");
  }
  DMAT(1); DMAT(2);
  glds16(wphi + head * 2048 + (w & 1) * 1024, l16, L0 + K2_PHI + (w & 1) * 1024);
  asm volatile("s_waitcnt vmcnt(5)\n\ts_barrier" : "+v"(qa[0]), "+v"(qa[1])::"memory");
  STAMP(1);
  int sidx = 0xDDDD; PIN(sidx);
  const unsigned one2 = 0x3f803f80u, sa = (lane & 4) ? 0u : one2, sb_ = (lane & 4) ? one2 : 0u;
  bf8 sel = asbf((u4){sa, sa, sb_, sb_}); PIN(sel);
  const lptr kvb = L + l16;
  f16v o = f16v{}, sA, sB, negm; f4 lsum = f4{};
  bf8 kA, kB, vA, vB;
#define KVR(i, j) ld128(kvb + ((i) & 15) * 4096 + (j) * 1024)
  kA = KVR(0, 0); kB = KVR(0, 1);
  sA = MFMA(kA, qa[0], f16v{}); sA = MFMA(kB, qa[1], sA);
  const float m0 = xhalf_max(max16(sA));
  negm = splat16(-m0); PIN(negm);
  sA = sA - m0;
  kA = KVR(1, 0); kB = KVR(1, 1); vA = KVR(0, 2); vB = vA;
  sB = f16v{};
#define EX(X, i) X[i] = __builtin_amdgcn_exp2f(X[i])
#define STEP(C, P, i, FIRST, HASK, HASK2, HASV) STEPD(C, P, i, FIRST, HASK, HASK2, HASV, NOP_)
#define STEPD(C, P, i, FIRST, HASK, HASK2, HASV, DM) do { \
    if (!(FIRST)) { o = SMF32(vB, hi8(P), o); } SB(); vB = KVR(i, 3); EX(C, 0); EX(C, 1); EX(C, 2); PIN(C); SB(); \
    if (!(FIRST)) { lsum = SMF16(sel, hi8(P), lsum); } SB(); EX(C, 3); EX(C, 4); PIN(C); SB(); \
    if (HASK) { P = MFMA(kA, qa[0], negm); } SB(); if (HASK2) kA = KVR((i) + 2, 0); EX(C, 5); EX(C, 6); EX(C, 7); PIN(C); SB(); \
    if (HASK) { P = MFMA(kB, qa[1], P); } SB(); DM; if (HASK2) kB = KVR((i) + 2, 1); EX(C, 8); EX(C, 9); EX(C, 10); PIN(C); SB(); \
    o = SMF32(vA, lo8(C), o); SB(); if (HASV) vA = KVR((i) + 1, 2); EX(C, 11); EX(C, 12); EX(C, 13); PIN(C); SB(); \
    lsum = SMF16(sel, lo8(C), lsum); SB(); EX(C, 14); EX(C, 15); PIN(C); SB(); } while (0)
  STEP(sA, sB, 0, true, true, true, true); STEP(sB, sA, 1, false, true, true, true);
  WAIT_BARV(3);
  STEPD(sA, sB, 2, false, true, true, true, DMAH(3, 0)); STEPD(sB, sA, 3, false, true, true, true, DMAH(3, 1024));
  STAMP(3);
  for (int T = 1; T < 13; T += 4) {
#pragma unroll
    for (int u = 0; u < 4; ++u) {
      const int i0 = 4 * (1 + u);
      STEP(sA, sB, i0, false, true, true, true); STEP(sB, sA, i0 + 1, false, true, true, true);
      WAIT_BARV(2);
      STEPD(sA, sB, i0 + 2, false, true, true, true, DMAH(T + u + 3, 0)); STEPD(sB, sA, i0 + 3, false, true, true, true, DMAH(T + u + 3, 1024));
    }
  }
  STAMP(4);
  STEP(sA, sB, 52, false, true, true, true); STEP(sB, sA, 53, false, true, true, true);
  WAIT_BARV(2);
  bf8 qb[2]; f4 part[8], zp[2];
  {
    const char* src = ws + WS_KVP + (size_t)head * 8 * KVP_STRIDE;
#pragma unroll
    for (int s = 0; s < 2; ++s) qb[s] = *(const bf8*)(ws + WS_QB + (size_t)(head * 64 + tb) * 2048 + s * 1024 + l16);
#pragma unroll
    for (int i = 0; i < 8; ++i) part[i] = *(const f4*)(src + i * KVP_STRIDE + w * 1024 + l16);
#pragma unroll
    for (int i = 0; i < 2; ++i) zp[i] = *(const f4*)(src + (i * 4 + (lane >> 4)) * KVP_STRIDE + 8192 + (lane & 15) * 16);
  }
  SB();
  STEP(sA, sB, 54, false, true, true, true); STEP(sB, sA, 55, false, true, true, true);
  STEP(sA, sB, 56, false, true, true, true); STEP(sB, sA, 57, false, true, true, true);
  WAIT_BARV(12);
  STEP(sA, sB, 58, false, true, true, true); STEP(sB, sA, 59, false, true, true, true);
  STEP(sA, sB, 60, false, true, true, true); STEP(sB, sA, 61, false, true, true, true);
  STEP(sA, sB, 62, false, true, false, true); STEP(sB, sA, 63, false, false, false, false);
  o = SMF32(vB, hi8(sB), o); lsum = SMF16(sel, hi8(sB), lsum);
  STAMP(5);
  float l = lsum[0];
  bool bad = !(l <= 1.8446744e19f);
#pragma unroll
  for (int i = 0; i < 16; ++i) bad |= !(__builtin_fabsf(o[i]) <= 3.0e38f);
  if (__builtin_expect(__any(bad), 0)) {
    float m = -__builtin_inff();
    for (int i = 0; i < 64; ++i) {
      const char* a = KV + i * 4096 + l16;
      f16v s = MFMA(*(const bf8*)a, qa[0], f16v{}); s = MFMA(*(const bf8*)(a + 1024), qa[1], s);
      m = __builtin_fmaxf(m, max16(s));
    }
    m = xhalf_max(m);
    negm = splat16(-m); o = f16v{}; lsum = f4{};
    for (int i = 0; i < 64; ++i) {
      const char* a = KV + i * 4096 + l16;
      f16v s = MFMA(*(const bf8*)a, qa[0], negm); s = MFMA(*(const bf8*)(a + 1024), qa[1], s);
#pragma unroll
      for (int j = 0; j < 16; ++j) EX(s, j);
      o = SMF32(*(const bf8*)(a + 2048), lo8(s), o); o = SMF32(*(const bf8*)(a + 3072), hi8(s), o);
      lsum = SMF16(sel, lo8(s), lsum); lsum = SMF16(sel, hi8(s), lsum);
    }
    l = lsum[0];
  }
  o *= __builtin_amdgcn_rcpf(l);
  char* od = ws + WS_O + (size_t)(tb * 32 + head) * 2048 + l16;
  *(u4*)od = pack8(o, 0); *(u4*)(od + 1024) = pack8(o, 1);
  STAMP(6);
  {
    const f4 a = ((part[0] + part[1]) + (part[2] + part[3])) + ((part[4] + part[5]) + (part[6] + part[7]));
    *(LDSP u2*)(L + K2_KV + (w >> 1) * 1024 + (g1 * 32 + (w & 1) * 16 + (lane & 15)) * 16 + h * 8) = (u2){pk2(a[0], a[1]), pk2(a[2], a[3])};
    if (w == 0) {
      f4 z = zp[0] + zp[1];
#pragma unroll
      for (int i = 0; i < 4; ++i) z[i] = xq_sum(z[i]);
      if (lane < 16) *(LDSP u2*)(L + K2_KV + 4096 + (lane >> 2) * 32 + (lane & 1) * 16 + ((lane >> 1) & 1) * 8) = (u2){pk2(z[0], z[1]), pk2(z[2], z[3])};
    }
    bf8 wf[2];
#pragma unroll
    for (int s = 0; s < 2; ++s) { const lptr a_ = L + K2_PHI + (8 * s + 4 * h + q) * 64 + g1 * 32 + p * 8; wf[s] = cat(ldtr(a_), ldtr(a_ + 1024)); }
    f16v pq = MFMA(wf[0], qb[0], f16v{});
    pq = MFMA(wf[1], qb[1], pq);
    f16v ep, em; float sp, sm;
    dual_exp(pq, ep, em, sp, sm);
    em *= sp * __builtin_amdgcn_rcpf(sm);
    asm volatile("s_waitcnt lgkmcnt(0)\n\ts_barrier" ::: "memory");
    f16v num = f16v{}, den = f16v{};
#pragma unroll
    for (int s = 0; s < 2; ++s) {
      const bf8 fp = asbf(pack8(ep, s)), fm = asbf(pack8(em, s));
      num = MFMA(ld128(L + K2_KV + s * 1024 + l16), fp, num);
      num = MFMA(ld128(L + K2_KV + 2048 + s * 1024 + l16), fm, num);
      den = MFMA(ld128(L + K2_KV + 4096 + s * 32 + h * 16), fp, den);
      den = MFMA(ld128(L + K2_KV + 4096 + 64 + s * 32 + h * 16), fm, den);
    }
    num *= __builtin_amdgcn_rcpf(den[0] + 1e-6f * sp);
    char* ob = ws + WS_O + (size_t)((64 + tb) * 32 + head) * 2048 + l16;
    *(u4*)ob = pack8(num, 0); *(u4*)(ob + 1024) = pack8(num, 1);
  }
  DIAG_END(1, ws);
#undef DMAT
#undef DMAH
#undef STEPD
#undef KVR
#undef EX
#undef STEP
}

constexpr int K3_LDS = 131072;
struct Frag3 { bf8 x0, x1; s4 w[2][2]; };

__global__ __launch_bounds__(256, 1) void k3_out(const char* __restrict__ woa, const char* __restrict__ wob, const char* __restrict__ ws, char* __restrict__ out) {
  __shared__ __attribute__((aligned(1024))) char smem[K3_LDS];
  DIAG_BEGIN;
  const lptr L = (lptr)smem;
  const unsigned L0 = (unsigned)(uintptr_t)L;
  const int tid = threadIdx.x, lane = tid & 63, w = __builtin_amdgcn_readfirstlane(tid >> 6);
  const int bid = blockIdx.x, xcd = bid & 7, br = xcd >> 2, mb = (xcd & 3) * 4 + ((bid >> 3) & 3), nb = bid >> 5;
  const char* O = ws + WS_O + (size_t)(br * 64 + mb * 4 + w) * 65536;
  const char* W = (br ? wob : woa) + nb * 256 + (size_t)w * 16 * 2048;
  const unsigned l16 = lane * 16;
  const unsigned woff = (lane >> 4) * 2048 + (((lane & 15) ^ ((lane >> 4) << 2)) << 4);
#define DMAO(t, i) glds16(O + (t) * 4096 + (i) * 1024, l16, L0 + ((t) & 3) * 32768 + w * 4096 + (i) * 1024)
#define DMAW(t, i) glds16(W + (size_t)(t) * (64 * 2048) + (i) * 8192, woff, L0 + ((t) & 3) * 32768 + 16384 + w * 4096 + (i) * 1024)
#define DMA_ALL(t) do { DMAO(t, 0); DMAW(t, 0); DMAO(t, 1); DMAW(t, 1); DMAO(t, 2); DMAW(t, 2); DMAO(t, 3); DMAW(t, 3); } while (0)
  DMA_ALL(0); DMA_ALL(1); DMA_ALL(2); DMA_ALL(3);
  const int wm = w & 1, wn = w >> 1, r = lane & 31, h = lane >> 5, g1 = (lane >> 4) & 1, q = (lane >> 2) & 3, p = lane & 3;
  lptr xa_[2], wa_[2][2];
#pragma unroll
  for (int half = 0; half < 2; ++half) {
    xa_[half] = L + half * 65536 + wm * 8192 + l16;
#pragma unroll
    for (int nt = 0; nt < 2; ++nt) wa_[half][nt] = L + half * 65536 + 16384 + (4 * h + q) * 256 + (((wn * 8 + nt * 4 + 2 * g1 + (p >> 1)) ^ (q << 2)) << 4) + 8 * (p & 1);
  }
  f16v acc[2][2];
#pragma unroll
  for (int i = 0; i < 2; ++i)
#pragma unroll
    for (int j = 0; j < 2; ++j) acc[i][j] = f16v{};
  Frag3 FA, FB;
#define RX(F, m, slot, ks) F.x##m = ld128(xa_[(slot) >> 1] + ((slot) & 1) * 32768 + (m) * 4096 + (ks) * 1024)
#define RW(F, nt, u, slot, ks) F.w[nt][u] = ldtr(wa_[(slot) >> 1][nt] + ((slot) & 1) * 32768 + (ks) * 4096 + (u) * 2048)
#define WF(F, nt) cat(F.w[nt][0], F.w[nt][1])
#define KSTEP(C, N, RD, slot, ks, D0, D1, D2, D3) do { \
    if (RD) { RW(N, 0, 0, slot, ks); RW(N, 0, 1, slot, ks); } D0; SB(); \
    acc[0][0] = MFMA(WF(C, 0), C.x0, acc[0][0]); SB(); if (RD) { RX(N, 0, slot, ks); RX(N, 1, slot, ks); } D1; SB(); \
    acc[1][0] = MFMA(WF(C, 0), C.x1, acc[1][0]); SB(); if (RD) { RW(N, 1, 0, slot, ks); RW(N, 1, 1, slot, ks); } D2; SB(); \
    acc[0][1] = MFMA(WF(C, 1), C.x0, acc[0][1]); SB(); D3; SB(); \
    acc[1][1] = MFMA(WF(C, 1), C.x1, acc[1][1]); SB(); } while (0)
#define D2_(t, i) do { DMAO(t, i); DMAW(t, i); } while (0)
#define STAGE(t, slot, WB, HASNEXT, HASDMA) do { \
    KSTEP(FA, FB, true, slot, 1, NOP_, NOP_, NOP_, NOP_); \
    KSTEP(FB, FA, true, slot, 2, NOP_, NOP_, NOP_, NOP_); \
    KSTEP(FA, FB, true, slot, 3, NOP_, NOP_, NOP_, NOP_); \
    WB; \
    if (HASDMA) KSTEP(FB, FA, true, ((slot) + 1) & 3, 0, D2_((t) + 4, 0), D2_((t) + 4, 1), D2_((t) + 4, 2), D2_((t) + 4, 3)); \
    else KSTEP(FB, FA, HASNEXT, ((slot) + 1) & 3, 0, NOP_, NOP_, NOP_, NOP_); } while (0)
  WAIT_BAR(24);
  STAMP(1);
  RW(FA, 0, 0, 0, 0); RW(FA, 0, 1, 0, 0); RX(FA, 0, 0, 0); RX(FA, 1, 0, 0); RW(FA, 1, 0, 0, 0); RW(FA, 1, 1, 0, 0);
  for (int t = 0; t < 12; t += 4) {
    STAGE(t, 0, WAIT_BAR(16), true, true); STAGE(t + 1, 1, WAIT_BAR(16), true, true);
    STAGE(t + 2, 2, WAIT_BAR(16), true, true); STAGE(t + 3, 3, WAIT_BAR(16), true, true);
  }
  STAMP(2);
  STAGE(12, 0, WAIT_BAR(16), true, false); STAGE(13, 1, WAIT_BAR(8), true, false);
  STAGE(14, 2, WAIT_BAR(0), true, false); STAGE(15, 3, NOP_, false, false);
  STAMP(3);
  const lptr st = L + w * 8192;
#pragma unroll
  for (int mt = 0; mt < 2; ++mt)
#pragma unroll
    for (int nt = 0; nt < 2; ++nt)
#pragma unroll
      for (int g = 0; g < 4; ++g)
        *(LDSP u2*)(st + (mt * 32 + r) * 128 + (((nt * 4 + g) ^ (r & 7)) << 4) + h * 8) = (u2){pk2(acc[mt][nt][4 * g], acc[mt][nt][4 * g + 1]), pk2(acc[mt][nt][4 * g + 2], acc[mt][nt][4 * g + 3])};
  asm volatile("s_waitcnt lgkmcnt(0)" ::: "memory");
  char* od = out + (size_t)(mb * 128 + wm * 64) * 4096 + br * 2048 + nb * 256 + wn * 128;
#pragma unroll
  for (int i = 0; i < 8; ++i) {
    const int row = i * 8 + (lane >> 3), ch = lane & 7;
    *(u4*)(od + (size_t)row * 4096 + ch * 16) = *(LDSP u4*)(st + row * 128 + ((ch ^ (row & 7)) << 4));
  }
  DIAG_END(2, ws);
}

extern "C" void kernel_launch(void* const* d_in, const int* in_sizes, int n_in, void* d_out, int out_size, void* d_ws, size_t ws_size, hipStream_t stream) {
  const char* xa = (const char*)d_in[0]; const char* wa = (const char*)d_in[1]; const char* woa = (const char*)d_in[2];
  const char* xb = (const char*)d_in[3]; const char* wb = (const char*)d_in[4]; const char* wphi = (const char*)d_in[5]; const char* wob = (const char*)d_in[6];
  char* ws = (char*)d_ws;
  k1_qkv<<<256, 512, 0, stream>>>(xa, wa, xb, wb, wphi, ws);
  k2_attn<<<256, 512, 0, stream>>>(wphi, ws);
  k3_out<<<256, 256, 0, stream>>>(woa, wob, ws, (char*)d_out);
}
```

```cpp
#include <hip/hip_runtime.h>
#include <hip/hip_bf16.h>
#include <stdint.h>

#define DI __device__ __forceinline__
#define LDSP __attribute__((address_space(3)))
typedef __bf16 bf2 __attribute__((ext_vector_type(2)));
typedef __bf16 bf8 __attribute__((ext_vector_type(8)));
typedef __bf16 bf16v __attribute__((ext_vector_type(16)));
typedef float f2 __attribute__((ext_vector_type(2)));
typedef float f4 __attribute__((ext_vector_type(4)));
typedef float f8 __attribute__((ext_vector_type(8)));
typedef float f16v __attribute__((ext_vector_type(16)));
typedef unsigned u2 __attribute__((ext_vector_type(2)));
typedef unsigned u4 __attribute__((ext_vector_type(4)));
typedef short s4 __attribute__((ext_vector_type(4)));
typedef short s8 __attribute__((ext_vector_type(8)));
typedef LDSP char* lptr;

#define SB() __builtin_amdgcn_sched_barrier(0)
#define PIN(x) asm volatile("" : "+v"(x))
#define MFMA(a, b, c) __builtin_amdgcn_mfma_f32_32x32x16_bf16((a), (b), (c), 0, 0, 0)
#define WAIT_BARV(N) asm volatile("s_waitcnt vmcnt(" #N ")\n\ts_barrier" ::: "memory")
#define WAIT_BAR(N) asm volatile("s_waitcnt vmcnt(" #N ") lgkmcnt(0)\n\ts_barrier" ::: "memory")

#define STAMP(i) (void)0
#define DIAG_BEGIN
#define DIAG_END(kid, wsp)

#define TS(idx) (void)0
#define TS_DUMP(wsp) (void)0
#define TS_PAD 0
constexpr float LOG2E = 1.4426950408889634f;
constexpr float QSCALE = 0.17677669529663687f * 1.4426950408889634f;

constexpr unsigned WS_FLAG = 0;
constexpr unsigned WS_QO = 262144;
constexpr unsigned QO_STRIDE = 4096 + 512;
constexpr unsigned WS_PO = WS_QO + (3u << 20);
constexpr unsigned PO_STRIDE = 8192 + 512;
constexpr unsigned WS_KVA = WS_PO + (5u << 20);
constexpr unsigned WS_KVP = WS_KVA + (8u << 20);
constexpr unsigned KVP_STRIDE = 8448;
constexpr unsigned WS_O = WS_KVP + (3u << 20);
constexpr unsigned WS_END = WS_O + (8u << 20);

DI unsigned pk2(float a, float b) { bf2 r = __builtin_convertvector((f2){a, b}, bf2); return __builtin_bit_cast(unsigned, r); }
DI u4 pack8(const f16v& x, int s) { return (u4){pk2(x[8 * s], x[8 * s + 1]), pk2(x[8 * s + 2], x[8 * s + 3]), pk2(x[8 * s + 4], x[8 * s + 5]), pk2(x[8 * s + 6], x[8 * s + 7])}; }
DI bf8 asbf(u4 v) { return __builtin_bit_cast(bf8, v); }
DI bf8 ld128(lptr p) { return *(const LDSP bf8*)p; }
DI s4 ldtr(lptr p) { return __builtin_amdgcn_ds_read_tr16_b64_v4i16((LDSP s4*)p); }
DI bf8 cat(s4 lo, s4 hi) { return __builtin_bit_cast(bf8, __builtin_shufflevector(lo, hi, 0, 1, 2, 3, 4, 5, 6, 7)); }
DI void glds16(const void* sbase, unsigned voff, unsigned lds) {
  asm volatile("s_mov_b32 m0, %2\n\ts_nop 0\n\tglobal_load_lds_dwordx4 %0, %1" ::"v"(voff), "s"(sbase), "s"(lds) : "memory");
}
DI float xhalf_max(float m) { auto rr = __builtin_amdgcn_permlane32_swap(__float_as_uint(m), __float_as_uint(m), false, false); return __builtin_fmaxf(__uint_as_float(rr[0]), __uint_as_float(rr[1])); }
DI float xhalf_sum(float m) { auto rr = __builtin_amdgcn_permlane32_swap(__float_as_uint(m), __float_as_uint(m), false, false); return __uint_as_float(rr[0]) + __uint_as_float(rr[1]); }
DI float max16(const f16v& p) {
  float a = __builtin_fmaxf(__builtin_fmaxf(p[0], p[1]), p[2]), b = __builtin_fmaxf(__builtin_fmaxf(p[3], p[4]), p[5]);
  a = __builtin_fmaxf(__builtin_fmaxf(a, p[6]), p[7]); b = __builtin_fmaxf(__builtin_fmaxf(b, p[8]), p[9]);
  a = __builtin_fmaxf(__builtin_fmaxf(a, p[10]), p[11]); b = __builtin_fmaxf(__builtin_fmaxf(b, p[12]), p[13]);
  a = __builtin_fmaxf(__builtin_fmaxf(a, p[14]), p[15]); return __builtin_fmaxf(a, b);
}

DI float min16(const f16v& p) {
  float a = __builtin_fminf(__builtin_fminf(p[0], p[1]), p[2]), b = __builtin_fminf(__builtin_fminf(p[3], p[4]), p[5]);
  a = __builtin_fminf(__builtin_fminf(a, p[6]), p[7]); b = __builtin_fminf(__builtin_fminf(b, p[8]), p[9]);
  a = __builtin_fminf(__builtin_fminf(a, p[10]), p[11]); b = __builtin_fminf(__builtin_fminf(b, p[12]), p[13]);
  a = __builtin_fminf(__builtin_fminf(a, p[14]), p[15]); return __builtin_fminf(a, b);
}
DI float sum16(const f16v& p) {
  const f8 a = __builtin_shufflevector(p, p, 0, 1, 2, 3, 4, 5, 6, 7) + __builtin_shufflevector(p, p, 8, 9, 10, 11, 12, 13, 14, 15);
  const f4 b = __builtin_shufflevector(a, a, 0, 1, 2, 3) + __builtin_shufflevector(a, a, 4, 5, 6, 7);
  const f2 c = __builtin_shufflevector(b, b, 0, 1) + __builtin_shufflevector(b, b, 2, 3);
  return c[0] + c[1];
}
DI f2 swap32(float a, float b) { auto rr = __builtin_amdgcn_permlane32_swap(__float_as_uint(a), __float_as_uint(b), false, false); return (f2){__uint_as_float(rr[0]), __uint_as_float(rr[1])}; }
template <class F> DI float dual_exp(const f16v& p, f16v& ep, f16v& em, F fill) {
  f2 t = swap32(max16(p), -min16(p));
  const float m = __builtin_fmaxf(t[0], t[1]) * LOG2E;
  t = swap32(m, m);
  ep = p * LOG2E - t[0]; em = p * -LOG2E - t[1];
#pragma unroll
  for (int r = 0; r < 16; ++r) {
    if (!(r & 1)) { PIN(ep); PIN(em); fill(r >> 1); }
    ep[r] = __builtin_amdgcn_exp2f(ep[r]); em[r] = __builtin_amdgcn_exp2f(em[r]);
  }
  t = swap32(sum16(ep), sum16(em));
  return t[0] + t[1];
}

constexpr int K1_W = 98304, K1_PHI = 147456, K1_PF = K1_PHI + 4096, L_VHI = K1_PF + 1024, K1_LDS = L_VHI + 8192 + TS_PAD;
struct Frag1 { bf8 x[4]; s4 w[3][2][2]; };
#define MFMA16(a, b, c) __builtin_amdgcn_mfma_f32_16x16x32_bf16((a), (b), (c), 0, 0, 0)
DI u4 pack44(const f4& a, const f4& b) { return (u4){pk2(a[0], a[1]), pk2(a[2], a[3]), pk2(b[0], b[1]), pk2(b[2], b[3])}; }
DI void glds4(const void* sbase, unsigned voff, unsigned lds) {
  asm volatile("s_mov_b32 m0, %2\n\ts_nop 0\n\tglobal_load_lds_dword %0, %1" ::"v"(voff), "s"(sbase), "s"(lds) : "memory");
}
DI float xq_sum(float m) { m = xhalf_sum(m); auto rr = __builtin_amdgcn_permlane16_swap(__float_as_uint(m), __float_as_uint(m), false, false); return __uint_as_float(rr[0]) + __uint_as_float(rr[1]); }

typedef unsigned long long u64;
extern "C" __device__ u64 launch_id() __asm("llvm.amdgcn.dispatch.id");
#define SMF32(a, b, c) __builtin_amdgcn_smfmac_f32_32x32x32_bf16((a), (b), (c), sidx, 0, 0)
#define SMF16(a, b, c) __builtin_amdgcn_smfmac_f32_16x16x64_bf16((a), (b), (c), sidx, 0, 0)
#define ST16(v, vo, so) do { const u4 d__ = (v); __builtin_amdgcn_raw_buffer_store_b128(d__, rs, (vo), (so), 16); asm volatile("s_nop 1" ::"v"(d__) : "memory"); } while (0)
#define LD16(vo, so) __builtin_amdgcn_raw_buffer_load_b128(rs, (vo), (so), 16)
#define BLD16(d, rd, vo, so, imm) asm volatile("buffer_load_dwordx4 %0, %1, %2, %3 offen offset:" #imm " sc1" : "=v"(d) : "v"(vo), "s"(rd), "s"(so) : "memory")
#define BLD8(d, rd, vo, so) asm volatile("buffer_load_dwordx2 %0, %1, %2, %3 offen sc1" : "=v"(d) : "v"(vo), "s"(rd), "s"(so) : "memory")
#define BLD4(d, rd, vo, so) asm volatile("buffer_load_dword %0, %1, %2, %3 offen sc1" : "=v"(d) : "v"(vo), "s"(rd), "s"(so) : "memory")
#define VM0() asm volatile("s_waitcnt vmcnt(0)" ::: "memory")
DI unsigned opq(unsigned x) { asm volatile("" : "+s"(x)); return x; }
DI bf16v lo8(const f16v& x) { return __builtin_bit_cast(bf16v, __builtin_shufflevector(x, x, 0, 1, 2, 3, 4, 5, 6, 7)); }
DI bf16v hi8(const f16v& x) { return __builtin_bit_cast(bf16v, __builtin_shufflevector(x, x, 8, 9, 10, 11, 12, 13, 14, 15)); }
DI f16v splat16(float v) { return (f16v){v, v, v, v, v, v, v, v, v, v, v, v, v, v, v, v}; }
DI f4 asf4(u4 v) { return __builtin_bit_cast(f4, v); }
DI u4 asu4(f4 v) { return __builtin_bit_cast(u4, v); }
DI f4 quad(const f16v& x, int i) { return (f4){x[4 * i], x[4 * i + 1], x[4 * i + 2], x[4 * i + 3]}; }
#define P_SLEEP 2
DI void spin(u64& cur, u64 epoch, u4 rd, unsigned vo, unsigned so) {
  for (unsigned n = 0; __builtin_expect(!__all(cur == epoch) && n < (1u << 18), 0); ++n) {
    __builtin_amdgcn_s_sleep(P_SLEEP);
    asm volatile("buffer_load_dwordx2 %0, %1, %2, %3 offen sc1\n\ts_waitcnt vmcnt(0)" : "=v"(cur) : "v"(vo), "s"(rd), "s"(so) : "memory");
  }
}
DI void swap16(bf8& a, bf8& b) {
  u4 x = __builtin_bit_cast(u4, a), y = __builtin_bit_cast(u4, b);
#pragma unroll
  for (int c = 0; c < 4; ++c) { auto rr = __builtin_amdgcn_permlane16_swap(x[c], y[c], false, false); x[c] = rr[0]; y[c] = rr[1]; }
  a = asbf(x); b = asbf(y);
}
constexpr int RING_X = 98304, L_QE = 98304, L_XCH = 0, L_KVI = 131072, L_PQ = 32768, KVI_STRIDE = 4352;
#define P_FLAG 0
#define P_BPOLL 6
#define P_POLL 8

__global__ __launch_bounds__(512, 2) void k12(const char* __restrict__ xa, const char* __restrict__ wa, const char* __restrict__ xb, const char* __restrict__ wb,
                                                 const char* __restrict__ wphi, char* __restrict__ ws) {
  __shared__ __attribute__((aligned(1024))) char smem[K1_LDS];
  DIAG_BEGIN;
  const lptr L = (lptr)smem;
  const unsigned L0 = (unsigned)(uintptr_t)L;
  const int tid = threadIdx.x, lane = tid & 63, w = __builtin_amdgcn_readfirstlane(tid >> 6);
  const int bid = blockIdx.x, xcd = bid & 7,
    br = (bid >> 5) & 1, hp = (xcd & 3) * 4 + ((bid >> 3) & 3), rb = (xcd >> 2) * 4 + (bid >> 6);
  const char* X = (br ? xb : xa) + (size_t)rb * 256 * 2048;
  const char* W = (br ? wb : wa) + hp * 128;
  unsigned xoff[4], woff[3];
#pragma unroll
  for (int i = 0; i < 4; ++i) { const int row = 32 * w + 8 * i + (lane >> 3); xoff[i] = row * 2048 + (((lane & 7) ^ ((row >> 1) & 7)) << 4); }
#pragma unroll
  for (int i = 0; i < 3; ++i) woff[i] = (8 * w + (lane >> 3)) * 6144 + i * 2048 + (((lane & 7) ^ ((((lane >> 4) & 1) | ((w & 1) << 1)) << 1)) << 4);
  const unsigned pfoff = (16 * (rb & 3) + 2 * w + ((lane >> 2) & 1)) * 6144 + ((lane & 3) == 3 ? 0 : (lane & 3)) * 2048;
#define DMAX(t, xs, i) glds16(X + (t) * 128, xoff[i], L0 + (xs) + (4 * w + (i)) * 1024)
#define DMAW(t, slot, i) glds16(W + (size_t)(t) * (64 * 6144), woff[i], L0 + K1_W + (slot) * 24576 + (i) * 8192 + w * 1024)
#define PFW(t) glds4(W + (size_t)(t) * (64 * 6144), pfoff, L0 + K1_PF)
  DMAW(0, 0, 0); DMAX(0, 0, 0); DMAX(0, 0, 1); DMAW(0, 0, 1); DMAX(0, 0, 2); DMAX(0, 0, 3); DMAW(0, 0, 2);
  PFW(2); PFW(3);
  if (br && w < 4) glds16(wphi + hp * 4096 + w * 1024, lane * 16, L0 + K1_PHI + w * 1024);
  DMAW(1, 1, 0); DMAW(1, 1, 1); DMAW(1, 1, 2);
  DMAX(1, 32768, 0); DMAX(1, 32768, 1); DMAX(1, 32768, 2); DMAX(1, 32768, 3);
  DMAX(2, 65536, 0); DMAX(2, 65536, 1); DMAX(2, 65536, 2); DMAX(2, 65536, 3);
  PFW(4);

  const int wm = w & 3, hh = w >> 2, t16 = lane & 15, g = lane >> 4, q = (lane >> 2) & 3, p = lane & 3;
  lptr xb_[2], wa_[2];
#pragma unroll
  for (int kk = 0; kk < 2; ++kk) xb_[kk] = L + (wm * 64 + t16) * 128 + (((g ^ (t16 >> 1)) ^ (4 * kk)) << 4);
#pragma unroll
  for (int j = 0; j < 2; ++j) wa_[j] = L + K1_W + (8 * g + q) * 128 + (((hh * 2 + j) ^ ((q >> 1) | ((g & 1) << 1))) << 5) + 8 * p;

  f4 acc[4][3][2];
#pragma unroll
  for (int i = 0; i < 4; ++i)
#pragma unroll
    for (int j = 0; j < 6; ++j) acc[i][j >> 1][j & 1] = f4{};
  Frag1 FA, FB;
#define RX(F, i, xp) F.x[i] = ld128((xp) + (i) * 2048)
#define RW(F, sg, j, slot, kk) do { F.w[sg][j][0] = ldtr(wa_[j] + (slot) * 24576 + (sg) * 8192 + (kk) * 4096); F.w[sg][j][1] = ldtr(wa_[j] + (slot) * 24576 + (sg) * 8192 + (kk) * 4096 + 512); } while (0)
#define WF(F, sg, j) cat(F.w[sg][j][0], F.w[sg][j][1])
#define MM(C, i, sg, j) do { if ((sg) < 2) acc[i][sg][j] = MFMA16(WF(C, sg, j), C.x[i], acc[i][sg][j]); else acc[i][sg][j] = MFMA16(C.x[i], WF(C, sg, j), acc[i][sg][j]); SB(); } while (0)
#define G4(C, sg, j, A0, A1, A2, A3) do { MM(C, 0, sg, j); A0; SB(); MM(C, 1, sg, j); A1; SB(); MM(C, 2, sg, j); A2; SB(); MM(C, 3, sg, j); A3; SB(); } while (0)
#define IFR(RD, x) do { if (RD) { x; } } while (0)
#define KSTEP(C, N, RD, xp, slot, kk, D0, D1, D2, D3) do { \
    G4(C, 0, 0, IFR(RD, RW(N, 0, 0, slot, kk)), IFR(RD, RX(N, 0, xp)), IFR(RD, RX(N, 1, xp)), D0); \
    G4(C, 0, 1, IFR(RD, RX(N, 2, xp)), IFR(RD, RX(N, 3, xp)), IFR(RD, RW(N, 0, 1, slot, kk)), NOP_); \
    G4(C, 1, 0, IFR(RD, RW(N, 1, 0, slot, kk)), D1, IFR(RD, RW(N, 1, 1, slot, kk)), NOP_); \
    G4(C, 1, 1, IFR(RD, RW(N, 2, 0, slot, kk)), NOP_, IFR(RD, RW(N, 2, 1, slot, kk)), D2); \
    G4(C, 2, 0, NOP_, NOP_, NOP_, NOP_); \
    G4(C, 2, 1, D3, NOP_, NOP_, NOP_); } while (0)
#define NOP_ (void)0
#define STAGE(t, slot, WB, HASNEXT, DX_PREV, DW, DX) do { \
    const lptr x1_ = xb_[1] + xs; \
    if (DX_PREV) KSTEP(FA, FB, true, x1_, slot, 1, DMAX((t) + 2, xp, 1), DMAX((t) + 2, xp, 2), DMAX((t) + 2, xp, 3), PFW((t) + 4 > 15 ? 15 : (t) + 4)); \
    else KSTEP(FA, FB, true, x1_, slot, 1, NOP_, NOP_, NOP_, NOP_); \
    xp = xs; xs = xs == 65536 ? 0 : xs + 32768; \
    const lptr x0_ = xb_[0] + xs; \
    WB; \
    if (DW) KSTEP(FB, FA, true, x0_, (slot) ^ 1, 0, DMAW((t) + 2, slot, 0), DMAW((t) + 2, slot, 1), DMAW((t) + 2, slot, 2), IFR(DX, DMAX((t) + 3, xp, 0))); \
    else KSTEP(FB, FA, HASNEXT, x0_, (slot) ^ 1, 0, NOP_, NOP_, NOP_, NOP_); } while (0)
  unsigned xs = 0, xp = 0;
  if (br && w < 4) WAIT_BAR(15); else WAIT_BAR(14);
  STAMP(1);
  RW(FA, 0, 0, 0, 0); RX(FA, 0, xb_[0]); RX(FA, 1, xb_[0]); RX(FA, 2, xb_[0]); RX(FA, 3, xb_[0]);
  RW(FA, 0, 1, 0, 0); RW(FA, 1, 0, 0, 0); RW(FA, 1, 1, 0, 0); RW(FA, 2, 0, 0, 0); RW(FA, 2, 1, 0, 0);
  STAGE(0, 0, WAIT_BAR(5), true, false, true, true);
  for (int t = 1; t < 13; t += 2) { STAGE(t, 1, WAIT_BAR(5), true, true, true, true); STAGE(t + 1, 0, WAIT_BAR(5), true, true, true, true); }
  STAGE(13, 1, WAIT_BAR(5), true, true, true, false);
  STAGE(14, 0, WAIT_BAR(0), true, false, false, false);
  STAGE(15, 1, NOP_, false, false, false, false);
  STAMP(2); TS(0);

  const __amdgpu_buffer_rsrc_t rs = __builtin_amdgcn_make_buffer_rsrc(ws, 0, WS_END, 0x00020000);
  const unsigned did = (unsigned)launch_id();
  const u64 epoch = ((u64)~did << 32) | did;
  const u4 rd = (u4){(unsigned)(uintptr_t)ws, (unsigned)((uintptr_t)ws >> 32) & 0xffffu, WS_END, 0x00020000u};
  const unsigned fl = WS_FLAG + hp * 1024;
  const int head = hp * 2 + hh, tb0 = rb * 8 + wm * 2, r = lane & 31, h = lane >> 5;
  const unsigned l16 = lane * 16, grp = (hp * 8 + rb) * 4 + wm;
  const unsigned koff = h * 512 + (((r >> 2) & 1) * 16 + (r >> 3) * 4 + (r & 3)) * 16;
  const unsigned KV = WS_KVA + (hp * 2 + br) * 64 * 4096 + w * 4096;
  int sidx = 0xDDDD; PIN(sidx);
  const unsigned one2 = 0x3f803f80u, sa = (lane & 4) ? 0u : one2, sb_ = (lane & 4) ? one2 : 0u;
  bf8 sel = asbf((u4){sa, sa, sb_, sb_}); PIN(sel);
  f16v o0 = f16v{}, o1 = f16v{}, sA, sB = f16v{}, nm0, nm1;
  f4 ls0 = f4{}, ls1 = f4{};
  bf8 q0[2], q1[2], kA, kB, vA, vB = bf8{}, ph[4][2];
  f4 part[8], stg[4];
  float mr0, mr1;
#define LDBLK(d, j) do { const unsigned s_ = KV + (((rb & 4) ^ 4) + (j)) * 32768; BLD16(d[0], rd, l16, s_, 0); BLD16(d[1], rd, l16, s_, 1024); BLD16(d[2], rd, l16, s_, 2048); BLD16(d[3], rd, l16, s_, 3072); } while (0)
#define LANDED(n, a) do { asm volatile("s_waitcnt vmcnt(" #n ")" : "+v"(part[0]), "+v"(part[1]), "+v"(part[2]), "+v"(part[3])::"memory"); \
    _Pragma("unroll") for (int k_ = 0; k_ < 4; ++k_) *(LDSP f4*)(L + (a) + k_ * 1024) = part[k_]; } while (0)
#define PRIO(u) (void)0
#define EX(X, i) X[i] = __builtin_amdgcn_exp2f(X[i])
#define EVEN(VB, K0, K1, H1, H2) do { \
    o1 = SMF32(vB, hi8(sB), o1); PIN(o1); SB(); vB = VB; EX(sA, 0); EX(sA, 1); EX(sA, 2); PIN(sA); SB(); \
    ls1 = SMF16(sel, hi8(sB), ls1); PIN(ls1); SB(); EX(sA, 3); EX(sA, 4); PIN(sA); SB(); \
    sB = MFMA(kA, q1[0], nm1); SB(); K0; EX(sA, 5); EX(sA, 6); EX(sA, 7); PIN(sA); SB(); \
    sB = MFMA(kB, q1[1], sB); PIN(sB); SB(); H1; K1; EX(sA, 8); EX(sA, 9); EX(sA, 10); PIN(sA); SB(); H2; SB(); \
    o0 = SMF32(vA, lo8(sA), o0); PIN(o0); SB(); EX(sA, 11); EX(sA, 12); EX(sA, 13); PIN(sA); SB(); \
    ls0 = SMF16(sel, lo8(sA), ls0); PIN(ls0); SB(); EX(sA, 14); EX(sA, 15); PIN(sA); SB(); } while (0)
#define ODD(VA, H1) do { \
    o0 = SMF32(vB, hi8(sA), o0); PIN(o0); SB(); EX(sB, 0); EX(sB, 1); EX(sB, 2); PIN(sB); SB(); \
    ls0 = SMF16(sel, hi8(sA), ls0); PIN(ls0); SB(); EX(sB, 3); EX(sB, 4); PIN(sB); SB(); \
    sA = MFMA(kA, q0[0], nm0); SB(); EX(sB, 5); EX(sB, 6); EX(sB, 7); PIN(sB); SB(); \
    sA = MFMA(kB, q0[1], sA); PIN(sA); SB(); H1; EX(sB, 8); EX(sB, 9); EX(sB, 10); PIN(sB); SB(); \
    o1 = SMF32(vA, lo8(sB), o1); PIN(o1); SB(); vA = VA; EX(sB, 11); EX(sB, 12); EX(sB, 13); PIN(sB); SB(); \
    ls1 = SMF16(sel, lo8(sB), ls1); PIN(ls1); SB(); EX(sB, 14); EX(sB, 15); PIN(sB); SB(); } while (0)

#define FLAG(kind, ln) do { const unsigned d_ = opq(did); __builtin_amdgcn_raw_buffer_store_b64((u2){d_, ~d_}, rs, (ln) ? ~0u : 0u, fl + (kind) * 16384 + rb * 128, 16); } while (0)
#define F1OFF(ln) (((ln) & 112) * 8)
  {
    const float qsc = br ? 1.0f : QSCALE;
#pragma unroll
    for (int i = 0; i < 4; ++i) { ph[i][0] = asbf(pack44(acc[i][1][0], acc[i][1][1])); ph[i][1] = asbf(pack44(acc[i & 2][2][i & 1], acc[(i & 2) + 1][2][i & 1])); }
    q0[0] = asbf(pack44(acc[0][0][0] * qsc, acc[0][0][1] * qsc)); q0[1] = asbf(pack44(acc[1][0][0] * qsc, acc[1][0][1] * qsc));
    q1[0] = asbf(pack44(acc[2][0][0] * qsc, acc[2][0][1] * qsc)); q1[1] = asbf(pack44(acc[3][0][0] * qsc, acc[3][0][1] * qsc));
    swap16(q0[0], q0[1]); swap16(q1[0], q1[1]);
  }
  u64 fw;
  f2 eps;
  int bsel = br;
  asm volatile("" : "+s"(bsel));
  if (bsel) {
    bf8 wf[2];
#pragma unroll
    for (int s = 0; s < 2; ++s) { const lptr a = L + K1_PHI + hh * 2048 + (8 * h + 4 * s + q) * 64 + (g & 1) * 32 + p * 8; wf[s] = cat(ldtr(a), ldtr(a + 1024)); }
    const lptr vw = L + (w < 6 ? K1_W + w * 4096 : L_VHI + (w - 6) * 4096) + l16;
#pragma unroll
    for (int i = 0; i < 4; ++i) *(LDSP bf8*)(vw + i * 1024) = ph[i][1];
#pragma unroll
    for (int m = 0; m < 2; ++m) {
      swap16(ph[2 * m][0], ph[2 * m + 1][0]);
      f16v e[2], pk = MFMA(wf[0], ph[2 * m][0], f16v{});
      pk = MFMA(wf[1], ph[2 * m + 1][0], pk);
      const float rs_ = __builtin_amdgcn_rcpf(dual_exp(pk, e[0], e[1], [](int) {}));
      const f2 rr = swap32(rs_, rs_);
      e[0] *= rr[0]; e[1] *= rr[1];
      const lptr row = L + 32768 + w * 8192 + (m * 32 + r) * 128;
#pragma unroll
      for (int c = 0; c < 8; ++c)
        *(LDSP u2*)(row + (((c >> 1) ^ ((r >> 1) & 3)) << 5) + ((((c & 1) * 2 + h) ^ ((r >> 2) & 2)) << 3)) =
            (u2){pk2(e[c >> 2][4 * (c & 3)], e[c >> 2][4 * (c & 3) + 1]), pk2(e[c >> 2][4 * (c & 3) + 2], e[c >> 2][4 * (c & 3) + 3])};
    }
    TS(1);
    asm volatile("s_waitcnt lgkmcnt(0)\n\ts_barrier" ::: "memory");
    TS(2);
    f4 kv0 = f4{}, kv1 = f4{}, zz = f4{};
    {
      const bf8 ones = asbf((u4){one2, one2, one2, one2});
      const lptr a = L + 32768 + hh * 32768 + (4 * g + q) * 128 + ((wm ^ (((g & 1) << 1) | (q >> 1))) << 5) + ((p ^ (g & 2)) << 3);
      const lptr vlo = L + K1_W + hh * 16384 + l16, vhi = L + (hh ? L_VHI : K1_W + 8192) + l16;
#pragma unroll
      for (int m = 0; m < 8; ++m) {
        if (m == P_BPOLL) BLD8(fw, rd, F1OFF(l16), fl);
        const bf8 f = cat(ldtr(a + m * 4096), ldtr(a + m * 4096 + 2048));
        const lptr v = (m < 4 ? vlo : vhi) + (m & 3) * 2048;
        kv0 = MFMA16(f, ld128(v), kv0); kv1 = MFMA16(f, ld128(v + 1024), kv1); zz = MFMA16(f, ones, zz);
      }
      PIN(kv0); PIN(kv1); PIN(zz);
    }
    STAMP(3); TS(3);
    asm volatile("s_barrier\n\ts_waitcnt vmcnt(0)" : "+v"(fw)::"memory");
    spin(fw, epoch, rd, F1OFF(l16), fl);
    STAMP(4); TS(4);
    const unsigned qs = WS_QO + grp * QO_STRIDE, so = WS_KVP + (head * 8 + rb) * KVP_STRIDE, s0 = KV + ((rb & 4) ^ 4) * 32768;
    const bf8 qx[2][2] = {{q0[0], q0[1]}, {q1[0], q1[1]}};
    u2 me;
    BLD16(part[0], rd, l16, s0, 0); BLD16(part[1], rd, l16, s0, 1024);
#pragma unroll
    for (int m = 0; m < 2; ++m) {
      f16v ep, em, pq = MFMA(wf[0], qx[m][0], f16v{});
      pq = MFMA(wf[1], qx[m][1], pq);
      const float sm_ = dual_exp(pq, ep, em, [&](int i) __attribute__((always_inline)) {
        if (m == 0) {
          if (i == 0) BLD16(part[2], rd, l16, s0, 2048);
          if (i == 1) BLD16(part[3], rd, l16, s0, 3072);
          if (i == 2) BLD16(q0[0], rd, l16, qs + hh * 2048, 0);
          if (i == 3) BLD16(q0[1], rd, l16, qs + hh * 2048, 1024);
          if (i == 4) BLD16(q1[0], rd, l16, qs + (hh ^ 1) * 2048, 0);
          if (i == 5) BLD16(q1[1], rd, l16, qs + (hh ^ 1) * 2048, 1024);
          if (i == 6) BLD8(me, rd, lane * 8, qs + 4096);
          if (i == 7) ST16(asu4(kv0), l16, so + wm * 2048);
        } else {
          if (i == 0) ST16(asu4(kv1), l16 + 1024, so + wm * 2048);
          if (i == 1) ST16(asu4(zz), t16 == 0 ? g * 16 : ~0u, so + 8192 + wm * 64);
        }
      });
      const f2 ss = swap32(sm_, sm_);
      em *= ss[0] * __builtin_amdgcn_rcpf(ss[1]); eps[m] = 1e-6f * ss[0];
      const lptr d = L + L_PQ + w * 8192 + m * 4096 + l16;
      *(LDSP u4*)d = pack8(ep, 0); *(LDSP u4*)(d + 1024) = pack8(ep, 1); *(LDSP u4*)(d + 2048) = pack8(em, 0); *(LDSP u4*)(d + 3072) = pack8(em, 1);
      if (m == 0) TS(5);
    }
    SB();
    TS(6);
    LANDED(0, w * 4096 + l16);
    LDBLK(stg, 1);
    asm volatile("" : "+v"(q0[0]), "+v"(q0[1]), "+v"(q1[0]), "+v"(q1[1]), "+v"(me));
    mr0 = __uint_as_float(hh ? me[1] : me[0]); mr1 = __uint_as_float(hh ? me[0] : me[1]);
    asm volatile("s_waitcnt lgkmcnt(0)\n\ts_barrier" ::: "memory");
    if (w == 0) FLAG(2, l16);
  }
  asm volatile("" : "+s"(bsel));
  if (!bsel) {
    const unsigned fk = (g & 1) * 1024 + (g >> 1) * 512 + t16 * 16, fv = 2048 + (g >> 1) * 1024 + (g & 1) * 512 + t16 * 16;
    const lptr img = L + 32768 + (wm >> 1) * 32768 + hh * 16384 + (wm & 1) * 8192;
    const unsigned so = WS_KVA + (head * 64 + tb0) * 4096, qs = WS_QO + grp * QO_STRIDE;
#pragma unroll
    for (int i = 0; i < 4; ++i) {
      ST16(__builtin_bit_cast(u4, ph[i][0]), fk + (i & 1) * 256, so + (i >> 1) * 4096); *(LDSP bf8*)(img + (i >> 1) * 4096 + fk + (i & 1) * 256) = ph[i][0];
      ST16(__builtin_bit_cast(u4, ph[i][1]), fv + (i & 1) * 256, so + (i >> 1) * 4096); *(LDSP bf8*)(img + (i >> 1) * 4096 + fv + (i & 1) * 256) = ph[i][1];
    }
    if (!hh) {
      const lptr qe = L + L_QE + wm * QO_STRIDE + l16;
      *(LDSP bf8*)qe = q0[0]; *(LDSP bf8*)(qe + 1024) = q0[1]; *(LDSP bf8*)(qe + 2048) = q1[0]; *(LDSP bf8*)(qe + 3072) = q1[1];
    } else {
      ST16(__builtin_bit_cast(u4, q0[0]), l16, qs); ST16(__builtin_bit_cast(u4, q0[1]), l16 + 1024, qs);
      ST16(__builtin_bit_cast(u4, q1[0]), l16 + 2048, qs); ST16(__builtin_bit_cast(u4, q1[1]), l16 + 3072, qs);
    }
    TS(1);
    asm volatile("s_waitcnt lgkmcnt(0)\n\ts_barrier" ::: "memory");
    TS(2);
    const lptr kd = L + 32768 + hh * 16384 + koff;
    kA = ld128(kd); kB = ld128(kd + 1024);
    f16v s0 = MFMA(kA, q0[0], f16v{}), s1 = MFMA(kA, q1[0], f16v{});
    s0 = MFMA(kB, q0[1], s0); s1 = MFMA(kB, q1[1], s1);
    mr0 = xhalf_max(max16(s0)); mr1 = xhalf_max(max16(s1));
    *(LDSP f2*)(L + L_QE + hh * 20480 + wm * QO_STRIDE + 4096 + lane * 8) = (f2){mr0, mr1};
    __builtin_amdgcn_raw_buffer_store_b64((u2){__float_as_uint(mr0), __float_as_uint(mr1)}, rs, hh ? lane * 8 : ~0u, qs + 4096, 16);
  }
  if (br) STAMP(5);
  TS(7);
  nm0 = splat16(-mr0); nm1 = splat16(-mr1); PIN(nm0); PIN(nm1);
  const unsigned ring = br ? 0 : 32768;
  unsigned kc = ring + hh * 16384 + koff, vc = ring + hh * 16384 + l16, wr = (ring ^ RING_X) + w * 4096 + l16;
  kA = ld128(L + kc); kB = ld128(L + kc + 1024); vA = ld128(L + vc + 2048);
  sA = MFMA(kA, q0[0], nm0); sA = MFMA(kB, q0[1], sA);
  float pl = 0.f;
  unsigned cb = br ? 1u << 18 : 1u, seq = ((0x3210u | ((rb + 1) & 3) << 16 | ((rb + 2) & 3) << 20 | ((rb + 3) & 3) << 24) ^ (rb & 4 ? 0x4440000u : 0x4444u)) >> (br * 8);
  const unsigned hm = hh ? ~0u : 0u;
#define INL(mask) ({ asm volatile("" : "+s"(cb)); (cb & (mask)) != 0; })
#define IN(mask) __builtin_expect(INL(mask), 0)
#define AB(b) (1u << ((b) + 2))
#define BB(b) (1u << ((b) + 18))
#define STG(k, imm) do { if (__builtin_expect(INL(0x00fc00fcu), 1)) { if ((k) == 0) asm volatile("s_waitcnt vmcnt(0)" : "+v"(stg[0]), "+v"(stg[1]), "+v"(stg[2]), "+v"(stg[3])::"memory"); \
      *(LDSP f4*)(L + wr + (k) * 1024) = stg[k]; BLD16(stg[k], rdk, l16, src, imm); } } while (0)
#define XBAR() (void)0
#define HOOKX hook7(1)
  const unsigned hookm0 = hh ? AB(0) : 0x00fc0000u | (w == 0 ? AB(1) : 0), hookm1 = hh ? 0x01f80000u : 0;
  do {
    u4 rdk = rd;
    const unsigned src = KV + (seq & 7) * 32768;
    rdk[2] = (cb & 0x007f007fu) ? WS_END : 0;
    auto hook = [&](int u) __attribute__((always_inline)) {
      if (u == (P_FLAG & 7) && IN(AB(P_FLAG / 8 - 2))) {
        unsigned ln = l16; PIN(ln);
        TS(5); VM0(); TS(6);
        asm volatile("s_barrier" ::: "memory");
        if (w == 0) FLAG(0, ln);
      }
      if (u == ((P_POLL - 1) & 7) && IN(AB((P_POLL - 1) / 8 - 2))) { unsigned ln = l16; PIN(ln); BLD8(fw, rd, F1OFF(ln), fl); }
      if (u == (P_POLL & 7) && IN(AB(-1))) {
        unsigned ln = l16; PIN(ln);
        TS(3);
        asm volatile("s_waitcnt vmcnt(0)" : "+v"(fw)::"memory");
        spin(fw, epoch, rd, F1OFF(ln), fl);
        STAMP(4); TS(4);
        LDBLK(part, 0); LDBLK(stg, 1);
      }
      if (u == 0 && IN(AB(0) & hm)) {
#pragma unroll
        for (int i = 0; i < 4; ++i) { part[i] = quad(o0, i); part[4 + i] = quad(o1, i); }
        eps = (f2){ls0[0], ls1[0]};
        o0 = f16v{}; o1 = f16v{}; ls0 = f4{}; ls1 = f4{};
      }
    };
    auto hookb = [&]() __attribute__((always_inline)) {
      TS(11 + (c & 15) * 6);
      if (IN(AB(-1))) {
        LANDED(4, wr);
        if (hh) {
          unsigned ln = l16; PIN(ln);
          const lptr qe = L + L_QE + wm * QO_STRIDE;
          q0[0] = ld128(qe + ln + 2048); q0[1] = ld128(qe + ln + 3072); q1[0] = ld128(qe + ln); q1[1] = ld128(qe + ln + 1024);
          const f2 me = *(LDSP f2*)(qe + 4096 + (ln >> 1));
          nm0 = splat16(-me[1]); nm1 = splat16(-me[0]); PIN(nm0); PIN(nm1);
        }
      }
      TS(12 + (c & 15) * 6);
      asm volatile("s_waitcnt lgkmcnt(0)\n\ts_barrier" ::: "memory");
      TS(13 + (c & 15) * 6);
      kc ^= RING_X; vc ^= RING_X; wr ^= RING_X;
      kA = ld128(L + kc); kB = ld128(L + kc + 1024);
    };
    auto hook7 = [&](int x) __attribute__((always_inline)) {
      if (!x) TS(8 + (c & 15) * 6);
#define BX(b) (BB(b) << x)
      if (IN(x ? hookm1 : hookm0)) {
        unsigned ln = l16; PIN(ln);
        const unsigned t_ = (ln >> 4) & 15, g_ = ln >> 8;
        const lptr kvi = L + L_KVI + hh * KVI_STRIDE;
        if (x && INL(BX(5))) VM0();
        asm volatile("" : "+v"(part[0]), "+v"(part[1]), "+v"(part[2]), "+v"(part[3]), "+v"(part[4]), "+v"(part[5]), "+v"(part[6]), "+v"(part[7]), "+v"(fw));
        if (!x && INL(AB(0))) {
#pragma unroll
          for (int i = 0; i < 4; ++i) { ST16(asu4(part[i]), ln + i * 1024, WS_PO + grp * PO_STRIDE); ST16(asu4(part[4 + i]), ln + i * 1024, WS_PO + grp * PO_STRIDE + 4096); }
          __builtin_amdgcn_raw_buffer_store_b32(__float_as_uint(eps[0]), rs, ln >> 2, WS_PO + grp * PO_STRIDE + 8192, 16);
          __builtin_amdgcn_raw_buffer_store_b32(__float_as_uint(eps[1]), rs, ln >> 2, WS_PO + grp * PO_STRIDE + 8192 + 256, 16);
        }
        if (!x && INL(AB(1))) FLAG(1, ln);
        if (INL(BX(0))) BLD8(fw, rd, F1OFF(ln), fl + 32768);
        if (INL(BX(1))) spin(fw, epoch, rd, F1OFF(ln), fl + 32768);
        if (INL(BX(2) | BX(3))) {
          const f4 a = ((part[0] + part[1]) + (part[2] + part[3])) + ((part[4] + part[5]) + (part[6] + part[7]));
          *(LDSP u2*)(kvi + wm * 1024 + ((g_ & 1) * 32 + (cb >> (21 + x)) * 16 + t_) * 16 + (g_ >> 1) * 8) = (u2){pk2(a[0], a[1]), pk2(a[2], a[3])};
        }
        if (INL(BX(1) | BX(2))) {
          const unsigned so = WS_KVP + head * 8 * KVP_STRIDE + wm * 2048 + (cb >> (20 + x)) * 1024;
#pragma unroll
          for (int i = 0; i < 8; ++i) BLD16(part[i], rd, ln, so + i * KVP_STRIDE, 0);
        }
        if (INL(BX(3))) {
          BLD16(part[0], rd, g_ * KVP_STRIDE + t_ * 16, WS_KVP + head * 8 * KVP_STRIDE + 8192, 0);
          BLD16(part[1], rd, g_ * KVP_STRIDE + t_ * 16, WS_KVP + (head * 8 + 4) * KVP_STRIDE + 8192, 0);
        }
        if (INL(BX(4))) {
          BLD8(fw, rd, ln & 0u, fl + 16384 + rb * 128);
          if (wm == 0) {
            f4 z = part[0] + part[1];
#pragma unroll
            for (int i = 0; i < 4; ++i) z[i] = xq_sum(z[i]);
            *(LDSP u2*)(kvi + 4096 + (t_ >> 2) * 32 + (t_ & 1) * 16 + ((t_ >> 1) & 1) * 8) = (u2){pk2(z[0], z[1]), pk2(z[2], z[3])};
          }
        }
        if (INL(BX(5))) {
          const lptr zi = kvi + 4096 + (g_ >> 1) * 16, pq = L + L_PQ + w * 8192 + ln;
#pragma unroll
          for (int m = 0; m < 2; ++m) {
            f16v den = f16v{}, num = f16v{};
#pragma unroll
            for (int j = 0; j < 4; ++j) { const bf8 f = ld128(pq + m * 4096 + j * 1024); den = MFMA(ld128(zi + j * 32), f, den); num = MFMA(ld128(kvi + ln + j * 1024), f, num); }
            num *= __builtin_amdgcn_rcpf(den[0] + eps[m]);
            ST16(pack8(num, 0), ln, WS_O + ((64 + tb0 + m) * 32 + head) * 2048); ST16(pack8(num, 1), ln + 1024, WS_O + ((64 + tb0 + m) * 32 + head) * 2048);
          }
          spin(fw, epoch, rd, ln & 0u, fl + 16384 + rb * 128);
          const unsigned so = WS_PO + grp * PO_STRIDE;
          BLD16(part[0], rd, ln, so + hh * 4096, 0); BLD16(part[1], rd, ln, so + hh * 4096, 1024); BLD16(part[2], rd, ln, so + hh * 4096, 2048); BLD16(part[3], rd, ln, so + hh * 4096, 3072);
          BLD4(pl, rd, ln >> 2, so + 8192 + hh * 256);
          asm volatile("" : "=v"(part[4]), "=v"(part[5]), "=v"(part[6]), "=v"(part[7]));
        }
      }
      if (!x) TS(9 + (c & 15) * 6);
    };
    EVEN(ld128(L + vc + 3072), kA = ld128(L + kc + 4096), kB = ld128(L + kc + 5120), hook(0), NOP_);
    ODD(ld128(L + vc + 6144), hook(1));
    EVEN(ld128(L + vc + 7168), kA = ld128(L + kc + 8192), kB = ld128(L + kc + 9216), hook(2); PRIO(2), XBAR());
    ODD(ld128(L + vc + 10240), STG(0, 0); hook(3); HOOKX; PRIO(3));
    EVEN(ld128(L + vc + 11264), kA = ld128(L + kc + 12288), kB = ld128(L + kc + 13312), STG(1, 1024); hook(4); PRIO(4), NOP_);
    ODD(ld128(L + vc + 14336), STG(2, 2048); hook(5));
    EVEN(ld128(L + vc + 15360), NOP_, NOP_, STG(3, 3072); hook(6), hookb(); PRIO(7));
    ODD(ld128(L + vc + 2048), hook(7); hook7(0));
    cb <<= 1; seq >>= 4;
  } while (!(cb & 0x02000200u));
  STAMP(6); TS(62);
  o1 = SMF32(vB, hi8(sB), o1); ls1 = SMF16(sel, hi8(sB), ls1);
  {
    const lptr xw = L + L_XCH + w * 4352, xr = L + L_XCH + (w ^ 4) * 4352;
#pragma unroll
    for (int i = 0; i < 4; ++i) *(LDSP f4*)(xw + i * 1024 + l16) = quad(o1, i);
    *(LDSP float*)(xw + 4096 + (l16 >> 2)) = ls1[0];
    asm volatile("s_waitcnt vmcnt(0) lgkmcnt(0)\n\ts_barrier" : "+v"(part[0]), "+v"(part[1]), "+v"(part[2]), "+v"(part[3]), "+v"(pl)::"memory");
#pragma unroll
    for (int i = 0; i < 4; ++i) {
      f4 t = *(LDSP f4*)(xr + i * 1024 + l16);
      if (br) t += part[i];
#pragma unroll
      for (int j = 0; j < 4; ++j) o0[4 * i + j] += t[j];
    }
    pl += *(LDSP float*)(xr + 4096 + (l16 >> 2));
  }
  float l = ls0[0] + pl;
  bool bad = !(l <= 1.8446744e19f);
#pragma unroll
  for (int i = 0; i < 16; ++i) bad |= !(__builtin_fabsf(o0[i]) <= 3.0e38f);
  if (__builtin_expect(__any(bad), 0)) {
    const unsigned kb = WS_KVA + (hp * 2 + br) * 64 * 4096;
    float m = -__builtin_inff();
    for (int i = 0; i < 64; ++i) {
      f16v s = MFMA(asbf(LD16(koff, kb + i * 4096)), q0[0], f16v{}); s = MFMA(asbf(LD16(koff + 1024, kb + i * 4096)), q0[1], s);
      m = __builtin_fmaxf(m, max16(s));
    }
    m = xhalf_max(m);
    nm0 = splat16(-m); o0 = f16v{}; ls0 = f4{};
    for (int i = 0; i < 64; ++i) {
      f16v s = MFMA(asbf(LD16(koff, kb + i * 4096)), q0[0], nm0); s = MFMA(asbf(LD16(koff + 1024, kb + i * 4096)), q0[1], s);
#pragma unroll
      for (int j = 0; j < 16; ++j) EX(s, j);
      o0 = SMF32(asbf(LD16(l16 + 2048, kb + i * 4096)), lo8(s), o0); o0 = SMF32(asbf(LD16(l16 + 3072, kb + i * 4096)), hi8(s), o0);
      ls0 = SMF16(sel, lo8(s), ls0); ls0 = SMF16(sel, hi8(s), ls0);
    }
    l = ls0[0];
  }
  o0 *= __builtin_amdgcn_rcpf(l);
  ST16(pack8(o0, 0), l16, WS_O + ((tb0 + hh) * 32 + hp * 2 + br) * 2048); ST16(pack8(o0, 1), l16 + 1024, WS_O + ((tb0 + hh) * 32 + hp * 2 + br) * 2048);
  TS(63); TS_DUMP(ws);
  DIAG_END(br, ws);
#undef DMAX
#undef DMAW
#undef PFW
#undef RX
#undef RW
#undef WF
#undef MM
#undef G4
#undef KSTEP
#undef STAGE
}

constexpr int K3_LDS = 131072;
struct Frag3 { bf8 x0, x1; s4 w[2][2]; };

__global__ __launch_bounds__(256, 1) void k3_out(const char* __restrict__ woa, const char* __restrict__ wob, const char* __restrict__ ws, char* __restrict__ out) {
  __shared__ __attribute__((aligned(1024))) char smem[K3_LDS];
  DIAG_BEGIN;
  const lptr L = (lptr)smem;
  const unsigned L0 = (unsigned)(uintptr_t)L;
  const int tid = threadIdx.x, lane = tid & 63, w = __builtin_amdgcn_readfirstlane(tid >> 6);
  const int bid = blockIdx.x, xcd = bid & 7, br = xcd >> 2, mb = (xcd & 3) * 4 + ((bid >> 3) & 3), nb = bid >> 5;
  const char* O = ws + WS_O + (size_t)(br * 64 + mb * 4 + w) * 65536;
  const char* W = (br ? wob : woa) + nb * 256 + (size_t)w * 16 * 2048;
  const unsigned l16 = lane * 16;
  const unsigned woff = (lane >> 4) * 2048 + (((lane & 15) ^ ((lane >> 4) << 2)) << 4);
#define DMAO(t, i) glds16(O + (t) * 4096 + (i) * 1024, l16, L0 + ((t) & 3) * 32768 + w * 4096 + (i) * 1024)
#define DMAW(t, i) glds16(W + (size_t)(t) * (64 * 2048) + (i) * 8192, woff, L0 + ((t) & 3) * 32768 + 16384 + w * 4096 + (i) * 1024)
#define DMA_ALL(t) do { DMAO(t, 0); DMAW(t, 0); DMAO(t, 1); DMAW(t, 1); DMAO(t, 2); DMAW(t, 2); DMAO(t, 3); DMAW(t, 3); } while (0)
  DMA_ALL(0); DMA_ALL(1); DMA_ALL(2); DMA_ALL(3);
  const int wm = w & 1, wn = w >> 1, r = lane & 31, h = lane >> 5, g1 = (lane >> 4) & 1, q = (lane >> 2) & 3, p = lane & 3;
  lptr xa_[2], wa_[2][2];
#pragma unroll
  for (int half = 0; half < 2; ++half) {
    xa_[half] = L + half * 65536 + wm * 8192 + l16;
#pragma unroll
    for (int nt = 0; nt < 2; ++nt) wa_[half][nt] = L + half * 65536 + 16384 + (4 * h + q) * 256 + (((wn * 8 + nt * 4 + 2 * g1 + (p >> 1)) ^ (q << 2)) << 4) + 8 * (p & 1);
  }
  f16v acc[2][2];
#pragma unroll
  for (int i = 0; i < 2; ++i)
#pragma unroll
    for (int j = 0; j < 2; ++j) acc[i][j] = f16v{};
  Frag3 FA, FB;
#define RX(F, m, slot, ks) F.x##m = ld128(xa_[(slot) >> 1] + ((slot) & 1) * 32768 + (m) * 4096 + (ks) * 1024)
#define RW(F, nt, u, slot, ks) F.w[nt][u] = ldtr(wa_[(slot) >> 1][nt] + ((slot) & 1) * 32768 + (ks) * 4096 + (u) * 2048)
#define WF(F, nt) cat(F.w[nt][0], F.w[nt][1])
#define KSTEP(C, N, RD, slot, ks, D0, D1, D2, D3) do { \
    if (RD) { RW(N, 0, 0, slot, ks); RW(N, 0, 1, slot, ks); } D0; SB(); \
    acc[0][0] = MFMA(WF(C, 0), C.x0, acc[0][0]); SB(); if (RD) { RX(N, 0, slot, ks); RX(N, 1, slot, ks); } D1; SB(); \
    acc[1][0] = MFMA(WF(C, 0), C.x1, acc[1][0]); SB(); if (RD) { RW(N, 1, 0, slot, ks); RW(N, 1, 1, slot, ks); } D2; SB(); \
    acc[0][1] = MFMA(WF(C, 1), C.x0, acc[0][1]); SB(); D3; SB(); \
    acc[1][1] = MFMA(WF(C, 1), C.x1, acc[1][1]); SB(); } while (0)
#define D2_(t, i) do { DMAO(t, i); DMAW(t, i); } while (0)
#define STAGE(t, slot, WB, HASNEXT, PD, HASDMA) do { \
    KSTEP(FA, FB, true, slot, 1, NOP_, IFR(PD, DMAO((t) + 3, 1)), NOP_, IFR(PD, DMAW((t) + 3, 1))); \
    KSTEP(FB, FA, true, slot, 2, NOP_, IFR(PD, DMAO((t) + 3, 2)), NOP_, IFR(PD, DMAW((t) + 3, 2))); \
    KSTEP(FA, FB, true, slot, 3, NOP_, IFR(PD, DMAO((t) + 3, 3)), NOP_, IFR(PD, DMAW((t) + 3, 3))); \
    WB; \
    KSTEP(FB, FA, HASNEXT, ((slot) + 1) & 3, 0, NOP_, IFR(HASDMA, DMAO((t) + 4, 0)), NOP_, IFR(HASDMA, DMAW((t) + 4, 0))); } while (0)
  WAIT_BAR(24);
  STAMP(1);
  RW(FA, 0, 0, 0, 0); RW(FA, 0, 1, 0, 0); RX(FA, 0, 0, 0); RX(FA, 1, 0, 0); RW(FA, 1, 0, 0, 0); RW(FA, 1, 1, 0, 0);
  STAGE(0, 0, WAIT_BAR(16), true, false, true);
  for (int t = 1; t < 9; t += 4) {
    STAGE(t, 1, WAIT_BAR(16), true, true, true); STAGE(t + 1, 2, WAIT_BAR(16), true, true, true);
    STAGE(t + 2, 3, WAIT_BAR(16), true, true, true); STAGE(t + 3, 0, WAIT_BAR(16), true, true, true);
  }
  STAGE(9, 1, WAIT_BAR(16), true, true, true); STAGE(10, 2, WAIT_BAR(16), true, true, true); STAGE(11, 3, WAIT_BAR(16), true, true, true);
  STAMP(2);
  STAGE(12, 0, WAIT_BAR(16), true, true, false); STAGE(13, 1, WAIT_BAR(8), true, false, false);
  STAGE(14, 2, WAIT_BAR(0), true, false, false); STAGE(15, 3, NOP_, false, false, false);
  STAMP(3);
  const lptr st = L + w * 8192;
#pragma unroll
  for (int mt = 0; mt < 2; ++mt)
#pragma unroll
    for (int nt = 0; nt < 2; ++nt)
#pragma unroll
      for (int g = 0; g < 4; ++g)
        *(LDSP u2*)(st + (mt * 32 + r) * 128 + (((nt * 4 + g) ^ (r & 7)) << 4) + h * 8) = (u2){pk2(acc[mt][nt][4 * g], acc[mt][nt][4 * g + 1]), pk2(acc[mt][nt][4 * g + 2], acc[mt][nt][4 * g + 3])};
  asm volatile("s_waitcnt lgkmcnt(0)" ::: "memory");
  char* od = out + (size_t)(mb * 128 + wm * 64) * 4096 + br * 2048 + nb * 256 + wn * 128;
#pragma unroll
  for (int i = 0; i < 8; ++i) {
    const int row = i * 8 + (lane >> 3), ch = lane & 7;
    *(u4*)(od + (size_t)row * 4096 + ch * 16) = *(LDSP u4*)(st + row * 128 + ((ch ^ (row & 7)) << 4));
  }
  DIAG_END(2, ws);
}

extern "C" void kernel_launch(void* const* d_in, const int* in_sizes, int n_in, void* d_out, int out_size, void* d_ws, size_t ws_size, hipStream_t stream) {
  const char* xa = (const char*)d_in[0]; const char* wa = (const char*)d_in[1]; const char* woa = (const char*)d_in[2];
  const char* xb = (const char*)d_in[3]; const char* wb = (const char*)d_in[4]; const char* wphi = (const char*)d_in[5]; const char* wob = (const char*)d_in[6];
  char* ws = (char*)d_ws;
  k12<<<256, 512, 0, stream>>>(xa, wa, xb, wb, wphi, ws);
  k3_out<<<256, 256, 0, stream>>>(woa, wob, ws, (char*)d_out);
}
```

```cpp
#include <hip/hip_runtime.h>
#include <hip/hip_bf16.h>
#include <stdint.h>

#define DI __device__ __forceinline__
#define LDSP __attribute__((address_space(3)))
typedef __bf16 bf2 __attribute__((ext_vector_type(2)));
typedef __bf16 bf8 __attribute__((ext_vector_type(8)));
typedef __bf16 bf16v __attribute__((ext_vector_type(16)));
typedef float f2 __attribute__((ext_vector_type(2)));
typedef float f4 __attribute__((ext_vector_type(4)));
typedef float f8 __attribute__((ext_vector_type(8)));
typedef float f16v __attribute__((ext_vector_type(16)));
typedef unsigned u2 __attribute__((ext_vector_type(2)));
typedef unsigned u4 __attribute__((ext_vector_type(4)));
typedef short s4 __attribute__((ext_vector_type(4)));
typedef short s8 __attribute__((ext_vector_type(8)));
typedef LDSP char* lptr;

#define SB() __builtin_amdgcn_sched_barrier(0)
#define PIN(x) asm volatile("" : "+v"(x))
#define MFMA(a, b, c) __builtin_amdgcn_mfma_f32_32x32x16_bf16((a), (b), (c), 0, 0, 0)
#define WAIT_BARV(N) asm volatile("s_waitcnt vmcnt(" #N ")\n\ts_barrier" ::: "memory")
#define WAIT_BAR(N) asm volatile("s_waitcnt vmcnt(" #N ") lgkmcnt(0)\n\ts_barrier" ::: "memory")

constexpr float LOG2E = 1.4426950408889634f;
constexpr float QSCALE = 0.17677669529663687f * 1.4426950408889634f;

constexpr unsigned WS_FLAG = 0;
constexpr unsigned WS_QO = 262144;
constexpr unsigned QO_STRIDE = 4096 + 512;
constexpr unsigned WS_PO = WS_QO + (3u << 20);
constexpr unsigned PO_STRIDE = 8192 + 512;
constexpr unsigned WS_KVA = WS_PO + (5u << 20);
constexpr unsigned WS_KVP = WS_KVA + (8u << 20);
constexpr unsigned KVP_STRIDE = 8448;
constexpr unsigned WS_O = WS_KVP + (3u << 20);
constexpr unsigned WS_END = WS_O + (8u << 20);

DI unsigned pk2(float a, float b) { bf2 r = __builtin_convertvector((f2){a, b}, bf2); return __builtin_bit_cast(unsigned, r); }
DI u4 pack8(const f16v& x, int s) { return (u4){pk2(x[8 * s], x[8 * s + 1]), pk2(x[8 * s + 2], x[8 * s + 3]), pk2(x[8 * s + 4], x[8 * s + 5]), pk2(x[8 * s + 6], x[8 * s + 7])}; }
DI bf8 asbf(u4 v) { return __builtin_bit_cast(bf8, v); }
DI bf8 ld128(lptr p) { return *(const LDSP bf8*)p; }
DI s4 ldtr(lptr p) { return __builtin_amdgcn_ds_read_tr16_b64_v4i16((LDSP s4*)p); }
DI bf8 cat(s4 lo, s4 hi) { return __builtin_bit_cast(bf8, __builtin_shufflevector(lo, hi, 0, 1, 2, 3, 4, 5, 6, 7)); }
DI void glds16(const void* sbase, unsigned voff, unsigned lds) {
  asm volatile("s_mov_b32 m0, %2\n\ts_nop 0\n\tglobal_load_lds_dwordx4 %0, %1" ::"v"(voff), "s"(sbase), "s"(lds) : "memory");
}
DI float xhalf_max(float m) { auto rr = __builtin_amdgcn_permlane32_swap(__float_as_uint(m), __float_as_uint(m), false, false); return __builtin_fmaxf(__uint_as_float(rr[0]), __uint_as_float(rr[1])); }
DI float xhalf_sum(float m) { auto rr = __builtin_amdgcn_permlane32_swap(__float_as_uint(m), __float_as_uint(m), false, false); return __uint_as_float(rr[0]) + __uint_as_float(rr[1]); }
DI float max16(const f16v& p) {
  float a = __builtin_fmaxf(__builtin_fmaxf(p[0], p[1]), p[2]), b = __builtin_fmaxf(__builtin_fmaxf(p[3], p[4]), p[5]);
  a = __builtin_fmaxf(__builtin_fmaxf(a, p[6]), p[7]); b = __builtin_fmaxf(__builtin_fmaxf(b, p[8]), p[9]);
  a = __builtin_fmaxf(__builtin_fmaxf(a, p[10]), p[11]); b = __builtin_fmaxf(__builtin_fmaxf(b, p[12]), p[13]);
  a = __builtin_fmaxf(__builtin_fmaxf(a, p[14]), p[15]); return __builtin_fmaxf(a, b);
}

DI float min16(const f16v& p) {
  float a = __builtin_fminf(__builtin_fminf(p[0], p[1]), p[2]), b = __builtin_fminf(__builtin_fminf(p[3], p[4]), p[5]);
  a = __builtin_fminf(__builtin_fminf(a, p[6]), p[7]); b = __builtin_fminf(__builtin_fminf(b, p[8]), p[9]);
  a = __builtin_fminf(__builtin_fminf(a, p[10]), p[11]); b = __builtin_fminf(__builtin_fminf(b, p[12]), p[13]);
  a = __builtin_fminf(__builtin_fminf(a, p[14]), p[15]); return __builtin_fminf(a, b);
}
DI float sum16(const f16v& p) {
  const f8 a = __builtin_shufflevector(p, p, 0, 1, 2, 3, 4, 5, 6, 7) + __builtin_shufflevector(p, p, 8, 9, 10, 11, 12, 13, 14, 15);
  const f4 b = __builtin_shufflevector(a, a, 0, 1, 2, 3) + __builtin_shufflevector(a, a, 4, 5, 6, 7);
  const f2 c = __builtin_shufflevector(b, b, 0, 1) + __builtin_shufflevector(b, b, 2, 3);
  return c[0] + c[1];
}
DI f2 swap32(float a, float b) { auto rr = __builtin_amdgcn_permlane32_swap(__float_as_uint(a), __float_as_uint(b), false, false); return (f2){__uint_as_float(rr[0]), __uint_as_float(rr[1])}; }
template <class F> DI float dual_exp(const f16v& p, f16v& ep, f16v& em, F fill) {
  f2 t = swap32(max16(p), -min16(p));
  const float m = __builtin_fmaxf(t[0], t[1]) * LOG2E;
  t = swap32(m, m);
  ep = p * LOG2E - t[0]; em = p * -LOG2E - t[1];
#pragma unroll
  for (int r = 0; r < 16; ++r) {
    if (!(r & 1)) { PIN(ep); PIN(em); fill(r >> 1); }
    ep[r] = __builtin_amdgcn_exp2f(ep[r]); em[r] = __builtin_amdgcn_exp2f(em[r]);
  }
  t = swap32(sum16(ep), sum16(em));
  return t[0] + t[1];
}

constexpr int K1_W = 98304, K1_PHI = 147456, K1_PF = K1_PHI + 4096, L_VHI = K1_PF + 1024, K1_LDS = L_VHI + 8192;
struct Frag1 { bf8 x[4]; s4 w[3][2][2]; };
#define MFMA16(a, b, c) __builtin_amdgcn_mfma_f32_16x16x32_bf16((a), (b), (c), 0, 0, 0)
DI u4 pack44(const f4& a, const f4& b) { return (u4){pk2(a[0], a[1]), pk2(a[2], a[3]), pk2(b[0], b[1]), pk2(b[2], b[3])}; }
DI void glds4(const void* sbase, unsigned voff, unsigned lds) {
  asm volatile("s_mov_b32 m0, %2\n\ts_nop 0\n\tglobal_load_lds_dword %0, %1" ::"v"(voff), "s"(sbase), "s"(lds) : "memory");
}
DI float xq_sum(float m) { m = xhalf_sum(m); auto rr = __builtin_amdgcn_permlane16_swap(__float_as_uint(m), __float_as_uint(m), false, false); return __uint_as_float(rr[0]) + __uint_as_float(rr[1]); }

typedef unsigned long long u64;
extern "C" __device__ u64 launch_id() __asm("llvm.amdgcn.dispatch.id");
#define SMF32(a, b, c) __builtin_amdgcn_smfmac_f32_32x32x32_bf16((a), (b), (c), sidx, 0, 0)
#define SMF16(a, b, c) __builtin_amdgcn_smfmac_f32_16x16x64_bf16((a), (b), (c), sidx, 0, 0)
#define ST16(v, vo, so) do { const u4 d__ = (v); __builtin_amdgcn_raw_buffer_store_b128(d__, rs, (vo), (so), 16); asm volatile("s_nop 1" ::"v"(d__) : "memory"); } while (0)
#define LD16(vo, so) __builtin_amdgcn_raw_buffer_load_b128(rs, (vo), (so), 16)
#define BLD16(d, rd, vo, so, imm) asm volatile("buffer_load_dwordx4 %0, %1, %2, %3 offen offset:" #imm " sc1" : "=v"(d) : "v"(vo), "s"(rd), "s"(so) : "memory")
#define BLD8(d, rd, vo, so) asm volatile("buffer_load_dwordx2 %0, %1, %2, %3 offen sc1" : "=v"(d) : "v"(vo), "s"(rd), "s"(so) : "memory")
#define BLD4(d, rd, vo, so) asm volatile("buffer_load_dword %0, %1, %2, %3 offen sc1" : "=v"(d) : "v"(vo), "s"(rd), "s"(so) : "memory")
#define VM0() asm volatile("s_waitcnt vmcnt(0)" ::: "memory")
DI unsigned opq(unsigned x) { asm volatile("" : "+s"(x)); return x; }
DI bf16v lo8(const f16v& x) { return __builtin_bit_cast(bf16v, __builtin_shufflevector(x, x, 0, 1, 2, 3, 4, 5, 6, 7)); }
DI bf16v hi8(const f16v& x) { return __builtin_bit_cast(bf16v, __builtin_shufflevector(x, x, 8, 9, 10, 11, 12, 13, 14, 15)); }
DI f16v splat16(float v) { return (f16v){v, v, v, v, v, v, v, v, v, v, v, v, v, v, v, v}; }
DI f4 asf4(u4 v) { return __builtin_bit_cast(f4, v); }
DI u4 asu4(f4 v) { return __builtin_bit_cast(u4, v); }
DI f4 quad(const f16v& x, int i) { return (f4){x[4 * i], x[4 * i + 1], x[4 * i + 2], x[4 * i + 3]}; }
DI void spin(u64& cur, u64 epoch, u4 rd, unsigned vo, unsigned so) {
  for (unsigned n = 0; __builtin_expect(!__all(cur == epoch) && n < (1u << 18), 0); ++n) {
    __builtin_amdgcn_s_sleep(2);
    asm volatile("buffer_load_dwordx2 %0, %1, %2, %3 offen sc1\n\ts_waitcnt vmcnt(0)" : "=v"(cur) : "v"(vo), "s"(rd), "s"(so) : "memory");
  }
}
DI void swap16(bf8& a, bf8& b) {
  u4 x = __builtin_bit_cast(u4, a), y = __builtin_bit_cast(u4, b);
#pragma unroll
  for (int c = 0; c < 4; ++c) { auto rr = __builtin_amdgcn_permlane16_swap(x[c], y[c], false, false); x[c] = rr[0]; y[c] = rr[1]; }
  a = asbf(x); b = asbf(y);
}
constexpr int RING_X = 98304, L_QE = 98304, L_XCH = 0, L_KVI = 131072, L_PQ = 32768, KVI_STRIDE = 4352;

__global__ __launch_bounds__(512, 2) void k12(const char* __restrict__ xa, const char* __restrict__ wa, const char* __restrict__ xb, const char* __restrict__ wb,
                                                 const char* __restrict__ wphi, const char*, const char*, char*) {
  __shared__ __attribute__((aligned(1024))) char smem[K1_LDS];
  const lptr L = (lptr)smem;
  const unsigned L0 = (unsigned)(uintptr_t)L;
  const int tid = threadIdx.x;
#define PFC() do { const char* pc_; unsigned n_; asm volatile("s_getpc_b64 %0\n\ts_mov_b32 %1, .Lk3_tail-." : "=s"(pc_), "=s"(n_)); \
    glds4(pc_, min((unsigned)tid * 128u, n_), L0 + K1_PF); } while (0)
  const int lane = tid & 63, w = __builtin_amdgcn_readfirstlane(tid >> 6);
  const int bid = blockIdx.x, xcd = bid & 7,
    br = (bid >> 5) & 1, hp = (xcd & 3) * 4 + ((bid >> 3) & 3), rb = (xcd >> 2) * 4 + (bid >> 6);
  unsigned xoff[4], woff[3];
#pragma unroll
  for (int i = 0; i < 4; ++i) { const int row = 32 * w + 8 * i + (lane >> 3); xoff[i] = row * 2048 + (((lane & 7) ^ ((row >> 1) & 7)) << 4); }
#pragma unroll
  for (int i = 0; i < 3; ++i) woff[i] = (8 * w + (lane >> 3)) * 6144 + i * 2048 + (((lane & 7) ^ ((((lane >> 4) & 1) | ((w & 1) << 1)) << 1)) << 4);
  PIN(xoff[0]); PIN(xoff[1]); PIN(xoff[2]); PIN(xoff[3]); PIN(woff[0]); PIN(woff[1]); PIN(woff[2]);
  SB();
  const char* X = (br ? xb : xa) + (size_t)rb * 256 * 2048;
  const char* W = (br ? wb : wa) + hp * 128;
  const unsigned pfoff = (16 * (rb & 3) + 2 * w + ((lane >> 2) & 1)) * 6144 + ((lane & 3) == 3 ? 0 : (lane & 3)) * 2048;
#define DMAX(t, xs, i) glds16(X + (t) * 128, xoff[i], L0 + (xs) + (4 * w + (i)) * 1024)
#define DMAW(t, slot, i) glds16(W + (size_t)(t) * (64 * 6144), woff[i], L0 + K1_W + (slot) * 24576 + (i) * 8192 + w * 1024)
#define PFW(t) glds4(W + (size_t)(t) * (64 * 6144), pfoff, L0 + K1_PF)
  const int tpf = (0x8520 >> (4 * (hp & 3))) & 15, s0_ = 4 + 3 * (hp & 3), ix = xcd | (bid >> 6) << 3;
  const char* const pfb = w < 3 ? X - (size_t)rb * 256 * 2048 + (s0_ + w) * 128 + ix * (64 * 2048) : W - hp * 128 + s0_ * (64 * 6144) + ix * 36864;
  const unsigned pfo = w < 3 ? lane * 2048 : min((w - 3) * 64 + lane, 287) * 128;
#define PFM(t, tw) glds4((t) == tpf ? pfb : W + (size_t)(tw) * (64 * 6144), (t) == tpf ? pfo : pfoff, L0 + K1_PF)
  DMAW(0, 0, 0); DMAX(0, 0, 0); DMAX(0, 0, 1); DMAW(0, 0, 1); DMAX(0, 0, 2); DMAX(0, 0, 3); DMAW(0, 0, 2);
  DMAW(1, 1, 0); DMAW(1, 1, 1); DMAW(1, 1, 2);
  DMAX(1, 32768, 0); DMAX(1, 32768, 1); DMAX(1, 32768, 2); DMAX(1, 32768, 3);

  const int wm = w & 3, hh = w >> 2, t16 = lane & 15, g = lane >> 4, q = (lane >> 2) & 3, p = lane & 3;
  lptr xb_[2], wa_[2];
#pragma unroll
  for (int kk = 0; kk < 2; ++kk) xb_[kk] = L + (wm * 64 + t16) * 128 + (((g ^ (t16 >> 1)) ^ (4 * kk)) << 4);
#pragma unroll
  for (int j = 0; j < 2; ++j) wa_[j] = L + K1_W + (8 * g + q) * 128 + (((hh * 2 + j) ^ ((q >> 1) | ((g & 1) << 1))) << 5) + 8 * p;

  f4 acc[4][3][2];
#pragma unroll
  for (int i = 0; i < 4; ++i)
#pragma unroll
    for (int j = 0; j < 6; ++j) acc[i][j >> 1][j & 1] = f4{};
  Frag1 FA, FB;
#define RX(F, i, xp) F.x[i] = ld128((xp) + (i) * 2048)
#define RW(F, sg, j, slot, kk) do { F.w[sg][j][0] = ldtr(wa_[j] + (slot) * 24576 + (sg) * 8192 + (kk) * 4096); F.w[sg][j][1] = ldtr(wa_[j] + (slot) * 24576 + (sg) * 8192 + (kk) * 4096 + 512); } while (0)
#define WF(F, sg, j) cat(F.w[sg][j][0], F.w[sg][j][1])
#define MM(C, i, sg, j) do { if ((sg) < 2) acc[i][sg][j] = MFMA16(WF(C, sg, j), C.x[i], acc[i][sg][j]); else acc[i][sg][j] = MFMA16(C.x[i], WF(C, sg, j), acc[i][sg][j]); SB(); } while (0)
#define G4(C, sg, j, A0, A1, A2, A3) do { MM(C, 0, sg, j); A0; SB(); MM(C, 1, sg, j); A1; SB(); MM(C, 2, sg, j); A2; SB(); MM(C, 3, sg, j); A3; SB(); } while (0)
#define IFR(RD, x) do { if (RD) { x; } } while (0)
#define KSTEP(C, N, RD, xp, slot, kk, D0, D1, D2, D3) do { \
    G4(C, 0, 0, IFR(RD, RW(N, 0, 0, slot, kk)), IFR(RD, RX(N, 0, xp)), IFR(RD, RX(N, 1, xp)), D0); \
    G4(C, 0, 1, IFR(RD, RX(N, 2, xp)), IFR(RD, RX(N, 3, xp)), IFR(RD, RW(N, 0, 1, slot, kk)), NOP_); \
    G4(C, 1, 0, IFR(RD, RW(N, 1, 0, slot, kk)), D1, IFR(RD, RW(N, 1, 1, slot, kk)), NOP_); \
    G4(C, 1, 1, IFR(RD, RW(N, 2, 0, slot, kk)), NOP_, IFR(RD, RW(N, 2, 1, slot, kk)), D2); \
    G4(C, 2, 0, NOP_, NOP_, NOP_, NOP_); \
    G4(C, 2, 1, D3, NOP_, NOP_, NOP_); } while (0)
#define NOP_ (void)0
#define STAGE(t, slot, WB, HASNEXT, DX_PREV, DW, DX, PF) do { \
    const lptr x1_ = xb_[1] + xs; \
    if (DX_PREV) KSTEP(FA, FB, true, x1_, slot, 1, DMAX((t) + 2, xp, 1), DMAX((t) + 2, xp, 2), DMAX((t) + 2, xp, 3), PF); \
    else KSTEP(FA, FB, true, x1_, slot, 1, NOP_, NOP_, NOP_, PF); \
    xp = xs; xs = xs == 65536 ? 0 : xs + 32768; \
    const lptr x0_ = xb_[0] + xs; \
    WB; \
    if (DW) KSTEP(FB, FA, true, x0_, (slot) ^ 1, 0, DMAW((t) + 2, slot, 0), DMAW((t) + 2, slot, 1), DMAW((t) + 2, slot, 2), IFR(DX, DMAX((t) + 3, xp, 0))); \
    else KSTEP(FB, FA, HASNEXT, x0_, (slot) ^ 1, 0, NOP_, NOP_, NOP_, NOP_); } while (0)
  unsigned xs = 0, xp = 0;
  WAIT_BAR(7);
  RW(FA, 0, 0, 0, 0); RX(FA, 0, xb_[0]); RX(FA, 1, xb_[0]); RX(FA, 2, xb_[0]); RX(FA, 3, xb_[0]);
  RW(FA, 0, 1, 0, 0); RW(FA, 1, 0, 0, 0); RW(FA, 1, 1, 0, 0); RW(FA, 2, 0, 0, 0); RW(FA, 2, 1, 0, 0);
#define PHI() glds16(wphi + hp * 4096 + (w & 3) * 1024, lane * 16, L0 + K1_PHI + (w & 3) * 1024)
  KSTEP(FA, FB, true, xb_[1], 0, 1, DMAX(2, 65536, 0); DMAX(2, 65536, 1), DMAX(2, 65536, 2); DMAX(2, 65536, 3), PFW(3); PFW(4), PHI(); PFM(0, 4));
  WAIT_BAR(8);
  xs = 32768;
  KSTEP(FB, FA, true, xb_[0] + 32768, 1, 0, DMAW(2, 0, 0), DMAW(2, 0, 1), DMAW(2, 0, 2), DMAX(3, 0, 0));
  for (int t = 1; t < 13; t += 2) { STAGE(t, 1, WAIT_BAR(5), true, true, true, true, PFM(t, t + 4)); STAGE(t + 1, 0, WAIT_BAR(5), true, true, true, true, PFM(t + 1, t + 5 > 15 ? 15 : t + 5)); }
  STAGE(13, 1, WAIT_BAR(5), true, true, true, false, PFC());
  typedef char* const __attribute__((address_space(4)))* argp;
  argp ka = (argp)__builtin_amdgcn_kernarg_segment_ptr();
  const char* wo;
  char* ws;
#define ARGS() do { asm volatile("" : "+s"(ka)); wo = ka[5 + (xcd >> 2)]; ws = ka[7]; } while (0)
  STAGE(14, 0, WAIT_BAR(0), true, false, false, false, ARGS());
  STAGE(15, 1, NOP_, false, false, false, false, NOP_);

  const __amdgpu_buffer_rsrc_t rs = __builtin_amdgcn_make_buffer_rsrc(ws, 0, WS_END, 0x00020000);
  const unsigned did = (unsigned)launch_id();
  const u64 epoch = ((u64)~did << 32) | did;
  const u4 rd = (u4){(unsigned)(uintptr_t)ws, (unsigned)((uintptr_t)ws >> 32) & 0xffffu, WS_END, 0x00020000u};
  const unsigned fl = WS_FLAG + hp * 1024;
  const int head = hp * 2 + hh, tb0 = rb * 8 + wm * 2, r = lane & 31, h = lane >> 5;
  const unsigned l16 = lane * 16, grp = (hp * 8 + rb) * 4 + wm;
  const unsigned koff = h * 512 + (((r >> 2) & 1) * 16 + (r >> 3) * 4 + (r & 3)) * 16;
  const unsigned KV = WS_KVA + (hp * 2 + br) * 64 * 4096 + w * 4096;
  int sidx = 0xDDDD; PIN(sidx);
  const unsigned one2 = 0x3f803f80u, sa = (lane & 4) ? 0u : one2, sb_ = (lane & 4) ? one2 : 0u;
  bf8 sel = asbf((u4){sa, sa, sb_, sb_}); PIN(sel);
  f16v o0 = f16v{}, o1 = f16v{}, sA, sB = f16v{}, nm0, nm1;
  f4 ls0 = f4{}, ls1 = f4{};
  bf8 q0[2], q1[2], kA, kB, vA, vB = bf8{}, ph[4][2];
  f4 part[8], stg[4];
  float mr0, mr1;
#define LDBLK(d, j) do { const unsigned s_ = KV + (((rb & 4) ^ 4) + (j)) * 32768; BLD16(d[0], rd, l16, s_, 0); BLD16(d[1], rd, l16, s_, 1024); BLD16(d[2], rd, l16, s_, 2048); BLD16(d[3], rd, l16, s_, 3072); } while (0)
#define LANDED(n, a) do { asm volatile("s_waitcnt vmcnt(" #n ")" : "+v"(part[0]), "+v"(part[1]), "+v"(part[2]), "+v"(part[3])::"memory"); \
    _Pragma("unroll") for (int k_ = 0; k_ < 4; ++k_) *(LDSP f4*)(L + (a) + k_ * 1024) = part[k_]; } while (0)
#define EX(X, i) X[i] = __builtin_amdgcn_exp2f(X[i])
#define EVEN(VB, K0, K1, H1, H2) do { \
    o1 = SMF32(vB, hi8(sB), o1); PIN(o1); SB(); vB = VB; EX(sA, 0); EX(sA, 1); EX(sA, 2); PIN(sA); SB(); \
    ls1 = SMF16(sel, hi8(sB), ls1); PIN(ls1); SB(); EX(sA, 3); EX(sA, 4); PIN(sA); SB(); \
    sB = MFMA(kA, q1[0], nm1); SB(); K0; EX(sA, 5); EX(sA, 6); EX(sA, 7); PIN(sA); SB(); \
    sB = MFMA(kB, q1[1], sB); PIN(sB); SB(); H1; K1; EX(sA, 8); EX(sA, 9); EX(sA, 10); PIN(sA); SB(); H2; SB(); \
    o0 = SMF32(vA, lo8(sA), o0); PIN(o0); SB(); EX(sA, 11); EX(sA, 12); EX(sA, 13); PIN(sA); SB(); \
    ls0 = SMF16(sel, lo8(sA), ls0); PIN(ls0); SB(); EX(sA, 14); EX(sA, 15); PIN(sA); SB(); } while (0)
#define ODD(VA, H1) do { \
    o0 = SMF32(vB, hi8(sA), o0); PIN(o0); SB(); EX(sB, 0); EX(sB, 1); EX(sB, 2); PIN(sB); SB(); \
    ls0 = SMF16(sel, hi8(sA), ls0); PIN(ls0); SB(); EX(sB, 3); EX(sB, 4); PIN(sB); SB(); \
    sA = MFMA(kA, q0[0], nm0); SB(); EX(sB, 5); EX(sB, 6); EX(sB, 7); PIN(sB); SB(); \
    sA = MFMA(kB, q0[1], sA); PIN(sA); SB(); H1; EX(sB, 8); EX(sB, 9); EX(sB, 10); PIN(sB); SB(); \
    o1 = SMF32(vA, lo8(sB), o1); PIN(o1); SB(); vA = VA; EX(sB, 11); EX(sB, 12); EX(sB, 13); PIN(sB); SB(); \
    ls1 = SMF16(sel, lo8(sB), ls1); PIN(ls1); SB(); EX(sB, 14); EX(sB, 15); PIN(sB); SB(); } while (0)

#define FLAG(kind, ln) do { const unsigned d_ = opq(did); __builtin_amdgcn_raw_buffer_store_b64((u2){d_, ~d_}, rs, (ln) ? ~0u : 0u, fl + (kind) * 16384 + rb * 128, 16); } while (0)
#define F1OFF(ln) (((ln) & 112) * 8)
  {
    const float qsc = br ? 1.0f : QSCALE;
#pragma unroll
    for (int i = 0; i < 4; ++i) { ph[i][0] = asbf(pack44(acc[i][1][0], acc[i][1][1])); ph[i][1] = asbf(pack44(acc[i & 2][2][i & 1], acc[(i & 2) + 1][2][i & 1])); }
    q0[0] = asbf(pack44(acc[0][0][0] * qsc, acc[0][0][1] * qsc)); q0[1] = asbf(pack44(acc[1][0][0] * qsc, acc[1][0][1] * qsc));
    q1[0] = asbf(pack44(acc[2][0][0] * qsc, acc[2][0][1] * qsc)); q1[1] = asbf(pack44(acc[3][0][0] * qsc, acc[3][0][1] * qsc));
    swap16(q0[0], q0[1]); swap16(q1[0], q1[1]);
  }
  u64 fw;
  f2 eps;
  int bsel = br;
  asm volatile("" : "+s"(bsel));
  if (bsel) {
    bf8 wf[2];
#pragma unroll
    for (int s = 0; s < 2; ++s) { const lptr a = L + K1_PHI + hh * 2048 + (8 * h + 4 * s + q) * 64 + (g & 1) * 32 + p * 8; wf[s] = cat(ldtr(a), ldtr(a + 1024)); }
    const lptr vw = L + (w < 6 ? K1_W + w * 4096 : L_VHI + (w - 6) * 4096) + l16;
#pragma unroll
    for (int i = 0; i < 4; ++i) *(LDSP bf8*)(vw + i * 1024) = ph[i][1];
#pragma unroll
    for (int m = 0; m < 2; ++m) {
      swap16(ph[2 * m][0], ph[2 * m + 1][0]);
      f16v e[2], pk = MFMA(wf[0], ph[2 * m][0], f16v{});
      pk = MFMA(wf[1], ph[2 * m + 1][0], pk);
      const float rs_ = __builtin_amdgcn_rcpf(dual_exp(pk, e[0], e[1], [](int) {}));
      const f2 rr = swap32(rs_, rs_);
      e[0] *= rr[0]; e[1] *= rr[1];
      const lptr row = L + 32768 + w * 8192 + (m * 32 + r) * 128;
#pragma unroll
      for (int c = 0; c < 8; ++c)
        *(LDSP u2*)(row + (((c >> 1) ^ ((r >> 1) & 3)) << 5) + ((((c & 1) * 2 + h) ^ ((r >> 2) & 2)) << 3)) =
            (u2){pk2(e[c >> 2][4 * (c & 3)], e[c >> 2][4 * (c & 3) + 1]), pk2(e[c >> 2][4 * (c & 3) + 2], e[c >> 2][4 * (c & 3) + 3])};
    }
    asm volatile("s_waitcnt lgkmcnt(0)\n\ts_barrier" ::: "memory");
    f4 kv0 = f4{}, kv1 = f4{}, zz = f4{};
    {
      const bf8 ones = asbf((u4){one2, one2, one2, one2});
      const lptr a = L + 32768 + hh * 32768 + (4 * g + q) * 128 + ((wm ^ (((g & 1) << 1) | (q >> 1))) << 5) + ((p ^ (g & 2)) << 3);
      const lptr vlo = L + K1_W + hh * 16384 + l16, vhi = L + (hh ? L_VHI : K1_W + 8192) + l16;
#pragma unroll
      for (int m = 0; m < 8; ++m) {
        if (m == 6) BLD8(fw, rd, F1OFF(l16), fl);
        const bf8 f = cat(ldtr(a + m * 4096), ldtr(a + m * 4096 + 2048));
        const lptr v = (m < 4 ? vlo : vhi) + (m & 3) * 2048;
        kv0 = MFMA16(f, ld128(v), kv0); kv1 = MFMA16(f, ld128(v + 1024), kv1); zz = MFMA16(f, ones, zz);
      }
      PIN(kv0); PIN(kv1); PIN(zz);
    }
    asm volatile("s_barrier\n\ts_waitcnt vmcnt(0)" : "+v"(fw)::"memory");
    spin(fw, epoch, rd, F1OFF(l16), fl);
    const unsigned qs = WS_QO + grp * QO_STRIDE, so = WS_KVP + (head * 8 + rb) * KVP_STRIDE, s0 = KV + ((rb & 4) ^ 4) * 32768;
    const bf8 qx[2][2] = {{q0[0], q0[1]}, {q1[0], q1[1]}};
    u2 me;
    BLD16(part[0], rd, l16, s0, 0); BLD16(part[1], rd, l16, s0, 1024);
#pragma unroll
    for (int m = 0; m < 2; ++m) {
      f16v ep, em, pq = MFMA(wf[0], qx[m][0], f16v{});
      pq = MFMA(wf[1], qx[m][1], pq);
      const float sm_ = dual_exp(pq, ep, em, [&](int i) __attribute__((always_inline)) {
        if (m == 0) {
          if (i == 0) BLD16(part[2], rd, l16, s0, 2048);
          if (i == 1) BLD16(part[3], rd, l16, s0, 3072);
          if (i == 2) BLD16(q0[0], rd, l16, qs + hh * 2048, 0);
          if (i == 3) BLD16(q0[1], rd, l16, qs + hh * 2048, 1024);
          if (i == 4) BLD16(q1[0], rd, l16, qs + (hh ^ 1) * 2048, 0);
          if (i == 5) BLD16(q1[1], rd, l16, qs + (hh ^ 1) * 2048, 1024);
          if (i == 6) BLD8(me, rd, lane * 8, qs + 4096);
          if (i == 7) ST16(asu4(kv0), l16, so + wm * 2048);
        } else {
          if (i == 0) ST16(asu4(kv1), l16 + 1024, so + wm * 2048);
          if (i == 1) ST16(asu4(zz), t16 == 0 ? g * 16 : ~0u, so + 8192 + wm * 64);
        }
      });
      const f2 ss = swap32(sm_, sm_);
      em *= ss[0] * __builtin_amdgcn_rcpf(ss[1]); eps[m] = 1e-6f * ss[0];
      const lptr d = L + L_PQ + w * 8192 + m * 4096 + l16;
      *(LDSP u4*)d = pack8(ep, 0); *(LDSP u4*)(d + 1024) = pack8(ep, 1); *(LDSP u4*)(d + 2048) = pack8(em, 0); *(LDSP u4*)(d + 3072) = pack8(em, 1);
    }
    SB();
    LANDED(0, w * 4096 + l16);
    LDBLK(stg, 1);
    asm volatile("" : "+v"(q0[0]), "+v"(q0[1]), "+v"(q1[0]), "+v"(q1[1]), "+v"(me));
    mr0 = __uint_as_float(hh ? me[1] : me[0]); mr1 = __uint_as_float(hh ? me[0] : me[1]);
    asm volatile("s_waitcnt lgkmcnt(0)\n\ts_barrier" ::: "memory");
    if (w == 0) FLAG(2, l16);
  }
  asm volatile("" : "+s"(bsel));
  if (!bsel) {
    const unsigned fk = (g & 1) * 1024 + (g >> 1) * 512 + t16 * 16, fv = 2048 + (g >> 1) * 1024 + (g & 1) * 512 + t16 * 16;
    const lptr img = L + 32768 + (wm >> 1) * 32768 + hh * 16384 + (wm & 1) * 8192;
    const unsigned so = WS_KVA + (head * 64 + tb0) * 4096, qs = WS_QO + grp * QO_STRIDE;
#pragma unroll
    for (int i = 0; i < 4; ++i) {
      ST16(__builtin_bit_cast(u4, ph[i][0]), fk + (i & 1) * 256, so + (i >> 1) * 4096); *(LDSP bf8*)(img + (i >> 1) * 4096 + fk + (i & 1) * 256) = ph[i][0];
      ST16(__builtin_bit_cast(u4, ph[i][1]), fv + (i & 1) * 256, so + (i >> 1) * 4096); *(LDSP bf8*)(img + (i >> 1) * 4096 + fv + (i & 1) * 256) = ph[i][1];
    }
    if (!hh) {
      const lptr qe = L + L_QE + wm * QO_STRIDE + l16;
      *(LDSP bf8*)qe = q0[0]; *(LDSP bf8*)(qe + 1024) = q0[1]; *(LDSP bf8*)(qe + 2048) = q1[0]; *(LDSP bf8*)(qe + 3072) = q1[1];
    } else {
      ST16(__builtin_bit_cast(u4, q0[0]), l16, qs); ST16(__builtin_bit_cast(u4, q0[1]), l16 + 1024, qs);
      ST16(__builtin_bit_cast(u4, q1[0]), l16 + 2048, qs); ST16(__builtin_bit_cast(u4, q1[1]), l16 + 3072, qs);
    }
    asm volatile("s_waitcnt lgkmcnt(0)\n\ts_barrier" ::: "memory");
    const lptr kd = L + 32768 + hh * 16384 + koff;
    kA = ld128(kd); kB = ld128(kd + 1024);
    f16v s0 = MFMA(kA, q0[0], f16v{}), s1 = MFMA(kA, q1[0], f16v{});
    s0 = MFMA(kB, q0[1], s0); s1 = MFMA(kB, q1[1], s1);
    mr0 = xhalf_max(max16(s0)); mr1 = xhalf_max(max16(s1));
    *(LDSP f2*)(L + L_QE + hh * 20480 + wm * QO_STRIDE + 4096 + lane * 8) = (f2){mr0, mr1};
    __builtin_amdgcn_raw_buffer_store_b64((u2){__float_as_uint(mr0), __float_as_uint(mr1)}, rs, hh ? lane * 8 : ~0u, qs + 4096, 16);
  }
  nm0 = splat16(-mr0); nm1 = splat16(-mr1); PIN(nm0); PIN(nm1);
  const unsigned ring = br ? 0 : 32768;
  unsigned kc = ring + hh * 16384 + koff, vc = ring + hh * 16384 + l16, wr = (ring ^ RING_X) + w * 4096 + l16;
  kA = ld128(L + kc); kB = ld128(L + kc + 1024); vA = ld128(L + vc + 2048);
  sA = MFMA(kA, q0[0], nm0); sA = MFMA(kB, q0[1], sA);
  float pl = 0.f;
  unsigned cb = br ? 1u << 18 : 1u, seq = ((0x3210u | ((rb + 1) & 3) << 16 | ((rb + 2) & 3) << 20 | ((rb + 3) & 3) << 24) ^ (rb & 4 ? 0x4440000u : 0x4444u)) >> (br * 8);
  const unsigned hm = hh ? ~0u : 0u;
#define INL(mask) ({ asm volatile("" : "+s"(cb)); (cb & (mask)) != 0; })
#define IN(mask) __builtin_expect(INL(mask), 0)
#define AB(b) (1u << ((b) + 2))
#define BB(b) (1u << ((b) + 18))
#define STG(k, imm) do { if (__builtin_expect(INL(0x00fc00fcu), 1)) { if ((k) == 0) asm volatile("s_waitcnt vmcnt(0)" : "+v"(stg[0]), "+v"(stg[1]), "+v"(stg[2]), "+v"(stg[3])::"memory"); \
      *(LDSP f4*)(L + wr + (k) * 1024) = stg[k]; BLD16(stg[k], rdk, l16, src, imm); } } while (0)
  const unsigned hookm0 = hh ? AB(0) : 0x00fc0000u | (w == 0 ? AB(1) : 0), hookm1 = hh ? 0x01f80000u : 0;
  const unsigned wom = (AB(2) | BB(2)) << wm;
  do {
    u4 rdk = rd;
    const unsigned src = KV + (seq & 7) * 32768;
    rdk[2] = (cb & 0x007f007fu) ? WS_END : 0;
    auto hook = [&](int u) __attribute__((always_inline)) {
      if (u == 0 && IN(AB(-2))) {
        unsigned ln = l16; PIN(ln);
        VM0();
        asm volatile("s_barrier" ::: "memory");
        if (w == 0) FLAG(0, ln);
      }
      if (u == 7 && IN(AB(-2))) { unsigned ln = l16; PIN(ln); BLD8(fw, rd, F1OFF(ln), fl); }
      if (u == 0 && IN(AB(-1))) {
        unsigned ln = l16; PIN(ln);
        asm volatile("s_waitcnt vmcnt(0)" : "+v"(fw)::"memory");
        spin(fw, epoch, rd, F1OFF(ln), fl);
        LDBLK(part, 0); LDBLK(stg, 1);
      }
      if (u == 4 && IN(wom)) {
        unsigned ln = l16; PIN(ln);
        glds4(wo + ((bid >> 3) * 8 + w) * 8192, ln * 8, L0 + K1_PF);
      }
      if (u == 0 && IN(AB(0) & hm)) {
#pragma unroll
        for (int i = 0; i < 4; ++i) { part[i] = quad(o0, i); part[4 + i] = quad(o1, i); }
        eps = (f2){ls0[0], ls1[0]};
        o0 = f16v{}; o1 = f16v{}; ls0 = f4{}; ls1 = f4{};
      }
    };
    auto hookb = [&]() __attribute__((always_inline)) {
      if (IN(AB(-1))) {
        LANDED(4, wr);
        if (hh) {
          unsigned ln = l16; PIN(ln);
          const lptr qe = L + L_QE + wm * QO_STRIDE;
          q0[0] = ld128(qe + ln + 2048); q0[1] = ld128(qe + ln + 3072); q1[0] = ld128(qe + ln); q1[1] = ld128(qe + ln + 1024);
          const f2 me = *(LDSP f2*)(qe + 4096 + (ln >> 1));
          nm0 = splat16(-me[1]); nm1 = splat16(-me[0]); PIN(nm0); PIN(nm1);
        }
      }
      asm volatile("s_waitcnt lgkmcnt(0)\n\ts_barrier" ::: "memory");
      kc ^= RING_X; vc ^= RING_X; wr ^= RING_X;
      kA = ld128(L + kc); kB = ld128(L + kc + 1024);
    };
    auto hook7 = [&](int x) __attribute__((always_inline)) {
#define BX(b) (BB(b) << x)
      if (IN(x ? hookm1 : hookm0)) {
        unsigned ln = l16; PIN(ln);
        const unsigned t_ = (ln >> 4) & 15, g_ = ln >> 8;
        const lptr kvi = L + L_KVI + hh * KVI_STRIDE;
        if (x && INL(BX(5))) VM0();
        asm volatile("" : "+v"(part[0]), "+v"(part[1]), "+v"(part[2]), "+v"(part[3]), "+v"(part[4]), "+v"(part[5]), "+v"(part[6]), "+v"(part[7]), "+v"(fw));
        if (!x && INL(AB(0))) {
#pragma unroll
          for (int i = 0; i < 4; ++i) { ST16(asu4(part[i]), ln + i * 1024, WS_PO + grp * PO_STRIDE); ST16(asu4(part[4 + i]), ln + i * 1024, WS_PO + grp * PO_STRIDE + 4096); }
          __builtin_amdgcn_raw_buffer_store_b32(__float_as_uint(eps[0]), rs, ln >> 2, WS_PO + grp * PO_STRIDE + 8192, 16);
          __builtin_amdgcn_raw_buffer_store_b32(__float_as_uint(eps[1]), rs, ln >> 2, WS_PO + grp * PO_STRIDE + 8192 + 256, 16);
        }
        if (!x && INL(AB(1))) FLAG(1, ln);
        if (INL(BX(0))) BLD8(fw, rd, F1OFF(ln), fl + 32768);
        if (INL(BX(1))) spin(fw, epoch, rd, F1OFF(ln), fl + 32768);
        if (INL(BX(2) | BX(3))) {
          const f4 a = ((part[0] + part[1]) + (part[2] + part[3])) + ((part[4] + part[5]) + (part[6] + part[7]));
          *(LDSP u2*)(kvi + wm * 1024 + ((g_ & 1) * 32 + (cb >> (21 + x)) * 16 + t_) * 16 + (g_ >> 1) * 8) = (u2){pk2(a[0], a[1]), pk2(a[2], a[3])};
        }
        if (INL(BX(1) | BX(2))) {
          const unsigned so = WS_KVP + head * 8 * KVP_STRIDE + wm * 2048 + (cb >> (20 + x)) * 1024;
#pragma unroll
          for (int i = 0; i < 8; ++i) BLD16(part[i], rd, ln, so + i * KVP_STRIDE, 0);
        }
        if (INL(BX(3))) {
          BLD16(part[0], rd, g_ * KVP_STRIDE + t_ * 16, WS_KVP + head * 8 * KVP_STRIDE + 8192, 0);
          BLD16(part[1], rd, g_ * KVP_STRIDE + t_ * 16, WS_KVP + (head * 8 + 4) * KVP_STRIDE + 8192, 0);
        }
        if (INL(BX(4))) {
          BLD8(fw, rd, ln & 0u, fl + 16384 + rb * 128);
          if (wm == 0) {
            f4 z = part[0] + part[1];
#pragma unroll
            for (int i = 0; i < 4; ++i) z[i] = xq_sum(z[i]);
            *(LDSP u2*)(kvi + 4096 + (t_ >> 2) * 32 + (t_ & 1) * 16 + ((t_ >> 1) & 1) * 8) = (u2){pk2(z[0], z[1]), pk2(z[2], z[3])};
          }
        }
        if (INL(BX(5))) {
          const lptr zi = kvi + 4096 + (g_ >> 1) * 16, pq = L + L_PQ + w * 8192 + ln;
#pragma unroll
          for (int m = 0; m < 2; ++m) {
            f16v den = f16v{}, num = f16v{};
#pragma unroll
            for (int j = 0; j < 4; ++j) { const bf8 f = ld128(pq + m * 4096 + j * 1024); den = MFMA(ld128(zi + j * 32), f, den); num = MFMA(ld128(kvi + ln + j * 1024), f, num); }
            num *= __builtin_amdgcn_rcpf(den[0] + eps[m]);
            ST16(pack8(num, 0), ln, WS_O + ((64 + tb0 + m) * 32 + head) * 2048); ST16(pack8(num, 1), ln + 1024, WS_O + ((64 + tb0 + m) * 32 + head) * 2048);
          }
          spin(fw, epoch, rd, ln & 0u, fl + 16384 + rb * 128);
          const unsigned so = WS_PO + grp * PO_STRIDE;
          BLD16(part[0], rd, ln, so + hh * 4096, 0); BLD16(part[1], rd, ln, so + hh * 4096, 1024); BLD16(part[2], rd, ln, so + hh * 4096, 2048); BLD16(part[3], rd, ln, so + hh * 4096, 3072);
          BLD4(pl, rd, ln >> 2, so + 8192 + hh * 256);
          asm volatile("" : "=v"(part[4]), "=v"(part[5]), "=v"(part[6]), "=v"(part[7]));
        }
      }
    };
    EVEN(ld128(L + vc + 3072), kA = ld128(L + kc + 4096), kB = ld128(L + kc + 5120), hook(0), NOP_);
    ODD(ld128(L + vc + 6144), hook(1));
    EVEN(ld128(L + vc + 7168), kA = ld128(L + kc + 8192), kB = ld128(L + kc + 9216), hook(2), NOP_);
    ODD(ld128(L + vc + 10240), STG(0, 0); hook(3); hook7(1));
    EVEN(ld128(L + vc + 11264), kA = ld128(L + kc + 12288), kB = ld128(L + kc + 13312), STG(1, 1024); hook(4), NOP_);
    ODD(ld128(L + vc + 14336), STG(2, 2048); hook(5));
    EVEN(ld128(L + vc + 15360), NOP_, NOP_, STG(3, 3072); hook(6), hookb());
    ODD(ld128(L + vc + 2048), hook(7); hook7(0));
    cb <<= 1; seq >>= 4;
  } while (!(cb & 0x02000200u));
  o1 = SMF32(vB, hi8(sB), o1); ls1 = SMF16(sel, hi8(sB), ls1);
  {
    const lptr xw = L + L_XCH + w * 4352, xr = L + L_XCH + (w ^ 4) * 4352;
#pragma unroll
    for (int i = 0; i < 4; ++i) *(LDSP f4*)(xw + i * 1024 + l16) = quad(o1, i);
    *(LDSP float*)(xw + 4096 + (l16 >> 2)) = ls1[0];
    asm volatile("s_waitcnt vmcnt(0) lgkmcnt(0)\n\ts_barrier" : "+v"(part[0]), "+v"(part[1]), "+v"(part[2]), "+v"(part[3]), "+v"(pl)::"memory");
#pragma unroll
    for (int i = 0; i < 4; ++i) {
      f4 t = *(LDSP f4*)(xr + i * 1024 + l16);
      if (br) t += part[i];
#pragma unroll
      for (int j = 0; j < 4; ++j) o0[4 * i + j] += t[j];
    }
    pl += *(LDSP float*)(xr + 4096 + (l16 >> 2));
  }
  float l = ls0[0] + pl;
  bool bad = !(l <= 1.8446744e19f);
#pragma unroll
  for (int i = 0; i < 16; ++i) bad |= !(__builtin_fabsf(o0[i]) <= 3.0e38f);
  if (__builtin_expect(__any(bad), 0)) {
    const unsigned kb = WS_KVA + (hp * 2 + br) * 64 * 4096;
    float m = -__builtin_inff();
    for (int i = 0; i < 64; ++i) {
      f16v s = MFMA(asbf(LD16(koff, kb + i * 4096)), q0[0], f16v{}); s = MFMA(asbf(LD16(koff + 1024, kb + i * 4096)), q0[1], s);
      m = __builtin_fmaxf(m, max16(s));
    }
    m = xhalf_max(m);
    nm0 = splat16(-m); o0 = f16v{}; ls0 = f4{};
    for (int i = 0; i < 64; ++i) {
      f16v s = MFMA(asbf(LD16(koff, kb + i * 4096)), q0[0], nm0); s = MFMA(asbf(LD16(koff + 1024, kb + i * 4096)), q0[1], s);
#pragma unroll
      for (int j = 0; j < 16; ++j) EX(s, j);
      o0 = SMF32(asbf(LD16(l16 + 2048, kb + i * 4096)), lo8(s), o0); o0 = SMF32(asbf(LD16(l16 + 3072, kb + i * 4096)), hi8(s), o0);
      ls0 = SMF16(sel, lo8(s), ls0); ls0 = SMF16(sel, hi8(s), ls0);
    }
    l = ls0[0];
  }
  o0 *= __builtin_amdgcn_rcpf(l);
  ST16(pack8(o0, 0), l16, WS_O + ((tb0 + hh) * 32 + hp * 2 + br) * 2048); ST16(pack8(o0, 1), l16 + 1024, WS_O + ((tb0 + hh) * 32 + hp * 2 + br) * 2048);
#undef DMAX
#undef DMAW
#undef PFW
#undef RX
#undef RW
#undef WF
#undef MM
#undef G4
#undef KSTEP
#undef STAGE
}

constexpr int K3_LDS = 131072;
struct Frag3 { bf8 x[4]; s4 w[4][2]; };

__global__ __launch_bounds__(512, 2) void k3_out(const char* __restrict__ woa, const char* __restrict__ wob, const char* __restrict__ ws, char* __restrict__ out) {
  __shared__ __attribute__((aligned(1024))) char smem[K3_LDS];
  const lptr L = (lptr)smem;
  const unsigned L0 = (unsigned)(uintptr_t)L;
  const int tid = threadIdx.x, lane = tid & 63, w8 = __builtin_amdgcn_readfirstlane(tid >> 6), w = w8 & 3;
  const int bid = blockIdx.x, xcd = bid & 7, br = xcd >> 2, mb = (xcd & 3) * 4 + ((bid >> 3) & 3), nb = bid >> 5;
  const char* O = ws + WS_O + (size_t)(br * 64 + mb * 4 + w) * 65536;
  const char* W = (br ? wob : woa) + nb * 256 + (size_t)w * 16 * 2048;
  const unsigned l16 = lane * 16;
  const unsigned woff = (lane >> 4) * 2048 + (((lane & 15) ^ ((lane >> 4) << 2)) << 4), woff1 = woff ^ 32;
#define DMAO(t, i) glds16(O + (t) * 4096 + (i) * 1024, l16, L0 + ((t) & 3) * 32768 + w * 4096 + (i) * 1024)
#define DMAW(t, i) glds16(W + (size_t)(t) * (64 * 2048) + (i) * 8192, (i) & 1 ? woff1 : woff, L0 + ((t) & 3) * 32768 + 16384 + w * 4096 + (i) * 1024)
#define DMA_ALL(t) do { DMAO(t, 0); DMAW(t, 0); DMAO(t, 1); DMAW(t, 1); DMAO(t, 2); DMAW(t, 2); DMAO(t, 3); DMAW(t, 3); } while (0)
  if (w8 >= 4) {
    const int wm = w & 1, wn = w >> 1, t_ = lane & 15, g = lane >> 4, q = (lane >> 2) & 3, p = lane & 3;
    lptr xa_[2], wa_[2][4];
#pragma unroll
    for (int half = 0; half < 2; ++half) {
      xa_[half] = L + half * 65536 + wm * 8192 + (g >> 1) * 1024 + ((g & 1) * 32 + t_) * 16;
#pragma unroll
      for (int ct = 0; ct < 4; ++ct) wa_[half][ct] = L + half * 65536 + 16384 + (16 * (g >> 1) + 4 * (g & 1) + q) * 256 + (((wn * 8 + 2 * ct + (p >> 1)) ^ (q << 2) ^ ((g & 1) << 1)) << 4) + 8 * (p & 1);
    }
    f4 acc[4][4];
#pragma unroll
    for (int i = 0; i < 4; ++i)
#pragma unroll
      for (int j = 0; j < 4; ++j) acc[i][j] = f4{};
    Frag3 FA, FB;
#define RX(F, tt, slot, hs) F.x[tt] = ld128(xa_[(slot) >> 1] + ((slot) & 1) * 32768 + ((tt) >> 1) * 4096 + ((tt) & 1) * 256 + (hs) * 2048)
#define RW(F, ct, u, slot, hs) F.w[ct][u] = ldtr(wa_[(slot) >> 1][ct] + ((slot) & 1) * 32768 + (hs) * 8192 + (u) * 2048)
#define WF(F, ct) cat(F.w[ct][0], F.w[ct][1])
#define MM(C, ct, tt, X) do { acc[ct][tt] = MFMA16(WF(C, ct), C.x[tt], acc[ct][tt]); SB(); X; SB(); } while (0)
#define KSTEP(C, N, RD, slot, hs) do { \
    MM(C, 0, 0, IFR(RD, RW(N, 0, 0, slot, hs))); MM(C, 0, 1, IFR(RD, RW(N, 0, 1, slot, hs))); MM(C, 0, 2, IFR(RD, RX(N, 0, slot, hs))); MM(C, 0, 3, IFR(RD, RX(N, 1, slot, hs))); \
    MM(C, 1, 0, IFR(RD, RX(N, 2, slot, hs))); MM(C, 1, 1, IFR(RD, RX(N, 3, slot, hs))); MM(C, 1, 2, IFR(RD, RW(N, 1, 0, slot, hs))); MM(C, 1, 3, IFR(RD, RW(N, 1, 1, slot, hs))); \
    MM(C, 2, 0, IFR(RD, RW(N, 2, 0, slot, hs))); MM(C, 2, 1, IFR(RD, RW(N, 2, 1, slot, hs))); MM(C, 2, 2, IFR(RD, RW(N, 3, 0, slot, hs))); MM(C, 2, 3, IFR(RD, RW(N, 3, 1, slot, hs))); \
    MM(C, 3, 0, NOP_); MM(C, 3, 1, NOP_); MM(C, 3, 2, NOP_); MM(C, 3, 3, NOP_); } while (0)
#define BARL() asm volatile("s_waitcnt lgkmcnt(0)\n\ts_barrier" ::: "memory")
#define STAGE(slot) do { KSTEP(FA, FB, true, slot, 1); BARL(); KSTEP(FB, FA, true, ((slot) + 1) & 3, 0); } while (0)
#pragma unroll
    for (int i = 0; i < 4; ++i) { PIN(wa_[0][i]); PIN(wa_[1][i]); PIN(acc[i][0]); PIN(acc[i][1]); PIN(acc[i][2]); PIN(acc[i][3]); }
    PIN(xa_[0]); PIN(xa_[1]);
    char* od = out + (size_t)(mb * 128 + wm * 64) * 4096 + br * 2048 + nb * 256 + wn * 128;
    asm volatile("" : "+s"(od));
    lptr sw[4], sr = L + w * 8192 + (lane >> 3) * 128 + (((lane & 7) ^ (lane >> 3)) << 4);
    unsigned oo = (lane >> 3) * 4096 + (lane & 7) * 16;
#pragma unroll
    for (int ct = 0; ct < 4; ++ct) { sw[ct] = L + w * 8192 + t_ * 128 + (((2 * ct + (g >> 1)) ^ (t_ & 7)) << 4) + (g & 1) * 8; PIN(sw[ct]); }
    PIN(sr); PIN(oo);
    BARL();
    RW(FA, 0, 0, 0, 0); RW(FA, 0, 1, 0, 0); RX(FA, 0, 0, 0); RX(FA, 1, 0, 0); RX(FA, 2, 0, 0); RX(FA, 3, 0, 0);
    RW(FA, 1, 0, 0, 0); RW(FA, 1, 1, 0, 0); RW(FA, 2, 0, 0, 0); RW(FA, 2, 1, 0, 0); RW(FA, 3, 0, 0, 0); RW(FA, 3, 1, 0, 0);
    for (int t = 0; t < 12; t += 4) { STAGE(0); STAGE(1); STAGE(2); STAGE(3); }
    STAGE(0); STAGE(1); STAGE(2); KSTEP(FA, FB, true, 3, 1);
    u4 y[8];
#define WR(ct, tt) *(LDSP u2*)(sw[ct] + (tt) * 2048) = (u2){pk2(acc[ct][tt][0], acc[ct][tt][1]), pk2(acc[ct][tt][2], acc[ct][tt][3])}
#define RY(i) do { asm volatile("" ::: "memory"); y[i] = *(LDSP u4*)(sr + (i) * 1024); } while (0)
#define SY(i) asm volatile("global_store_dwordx4 %0, %1, %2 nt" ::"v"(oo), "v"(y[i]), "s"(od + (i) * 32768) : "memory")
    MM(FB, 0, 0, NOP_); MM(FB, 1, 0, NOP_); MM(FB, 2, 0, NOP_); MM(FB, 3, 0, NOP_);
    MM(FB, 0, 1, NOP_); MM(FB, 1, 1, WR(0, 0)); MM(FB, 2, 1, WR(1, 0)); MM(FB, 3, 1, WR(2, 0));
    MM(FB, 0, 2, WR(3, 0); RY(0)); MM(FB, 1, 2, WR(0, 1); RY(1)); MM(FB, 2, 2, WR(1, 1)); MM(FB, 3, 2, WR(2, 1));
    MM(FB, 0, 3, WR(3, 1); RY(2)); MM(FB, 1, 3, WR(0, 2); RY(3)); MM(FB, 2, 3, WR(1, 2)); MM(FB, 3, 3, WR(2, 2));
    WR(3, 2); RY(4); RY(5); SB(); WR(0, 3); WR(1, 3); WR(2, 3); WR(3, 3); RY(6); RY(7); SB();
    SY(0); SY(1); SY(2); SY(3); SY(4); SY(5); SY(6); SY(7);
    asm volatile(".Lk3_tail:" ::: "memory");
    return;
  }
  DMA_ALL(0); DMA_ALL(1); DMA_ALL(2); DMA_ALL(3);
  WAIT_BARV(24);
  for (int t = 4; t < 16; ++t) { WAIT_BARV(16); DMA_ALL(t); }
  WAIT_BARV(16); WAIT_BARV(8); WAIT_BARV(0);
}

extern "C" void kernel_launch(void* const* d_in, const int* in_sizes, int n_in, void* d_out, int out_size, void* d_ws, size_t ws_size, hipStream_t stream) {
  const char* xa = (const char*)d_in[0]; const char* wa = (const char*)d_in[1]; const char* woa = (const char*)d_in[2];
  const char* xb = (const char*)d_in[3]; const char* wb = (const char*)d_in[4]; const char* wphi = (const char*)d_in[5]; const char* wob = (const char*)d_in[6];
  char* ws = (char*)d_ws;
  k12<<<256, 512, 0, stream>>>(xa, wa, xb, wb, wphi, woa, wob, ws);
  k3_out<<<256, 512, 0, stream>>>(woa, wob, ws, (char*)d_out);
}
```
